```python
import torch

def _softmax(x, axis=-1):
    return torch.softmax(x, dim=axis)
def _gelu(x):
    return torch.nn.functional.gelu(x, approximate="tanh")
def _silu(x):
    return torch.nn.functional.silu(x)
def _sigmoid(x):
    return torch.sigmoid(x)
def _layernorm(x, axis=-1, eps=1e-5):
    m = x.mean(dim=axis, keepdim=True)
    v = x.var(dim=axis, keepdim=True, unbiased=False)
    return (x - m) / torch.sqrt(v + eps)
def _scatter_add(tgt, idx, src, axis=0):
    return tgt.clone().index_add_(axis, idx, src)
def _segment_sum(src, seg, n):
    out = torch.zeros((n,) + src.shape[1:], dtype=src.dtype)
    return out.index_add_(0, seg, src)
def _pad(x, pw):
    flat = tuple(v for pair in reversed(pw) for v in pair)
    return torch.nn.functional.pad(x, flat)

kernel_name = 'sequential_preLN__spectral_gfnet__dense_activation_squared_relu__prompt'

def setup_inputs():

    _w = (lambda a: (lambda t: t.to(torch.bfloat16) if t.is_floating_point() else t)(torch.as_tensor(a)))
    def _A_setup():
        B, S, D = 1, 2048, 512
        Df = D // 2 + 1
        return {
            "x": _w((lambda *s: torch.randn(*s, dtype=torch.bfloat16) * 0.05)(B, S, D)),
            "w_r": _w((lambda *s: torch.randn(*s, dtype=torch.bfloat16) * 0.4)(S, Df) * 0.02),
            "w_i": _w((lambda *s: torch.randn(*s, dtype=torch.bfloat16) * 0.4)(S, Df) * 0.02),
        }
    _A_in = _A_setup()

    def _B_setup():
        B, S, D, F = 1, 2048, 512, 1376
        return {
            "x": _w((lambda *s: torch.randn(*s, dtype=torch.bfloat16) * 0.05)(B, S, D)),
            "w1": _w((lambda *s: torch.randn(*s, dtype=torch.bfloat16) * 0.05)(D, F)),
            "b1": _w((lambda *s: torch.randn(*s, dtype=torch.bfloat16) * 0.2)(F)),
            "w2": _w((lambda *s: torch.randn(*s, dtype=torch.bfloat16) * 0.05)(F, D)),
            "b2": _w((lambda *s: torch.randn(*s, dtype=torch.bfloat16) * 0.8)(D)),
        }
    _B_in = _B_setup()

    return {
        "A_x": _A_in["x"],
        "A_w_r": _A_in["w_r"],
        "A_w_i": _A_in["w_i"],
        "B_x": _B_in["x"],
        "B_w1": _B_in["w1"],
        "B_b1": _B_in["b1"],
        "B_w2": _B_in["w2"],
        "B_b2": _B_in["b2"],
    }

def reference(A_x, A_w_r, A_w_i, B_x, B_w1, B_b1, B_w2, B_b2):
    def _A_ref(x, w_r, w_i):
        B, S, D = x.shape
        X = (lambda x, axes=(-2,-1): torch.fft.rfft2(x.float(), dim=axes))(x, axes=(-2, -1))
        W = w_r + 1j * w_i
        X = X * W
        y = (lambda x, s, axes=(-2,-1): torch.fft.irfft2(x, s=s, dim=axes).to(torch.bfloat16))(X, (S, D), axes=(-2, -1))
        return y

    def _B_ref(x, w1, b1, w2, b2):
        h = torch.einsum("bsd,df->bsf", x, w1) + b1
        zero = torch.zeros_like(h)
        r = torch.where(h > zero, h, zero)
        h = r * r
        out = torch.einsum("bsf,fd->bsd", h, w2) + b2
        return out

    x = A_x
    h = x + _A_ref(_layernorm(x, axis=-1), A_w_r, A_w_i)
    h2 = h + _B_ref(_layernorm(h, axis=-1), B_w1, B_b1, B_w2, B_b2)
    return h2
```

```cpp
#include <hip/hip_runtime.h>
#include <hip/hip_bf16.h>
#include <stdint.h>

typedef __attribute__((ext_vector_type(8))) __bf16 bf16x8;
typedef __attribute__((ext_vector_type(2))) __bf16 bf16x2;
typedef __attribute__((ext_vector_type(4))) short s16x4;
typedef __attribute__((ext_vector_type(4))) float f32x4;
typedef __attribute__((ext_vector_type(16))) float f32x16;
typedef __attribute__((ext_vector_type(4))) unsigned u32x4;
typedef __attribute__((ext_vector_type(2))) unsigned u32x2;
typedef unsigned short u16;
#define LAS __attribute__((address_space(3)))
#define DEV __device__ __forceinline__

#define STAMP(k, i)

constexpr int S_ = 2048, D_ = 512, F_ = 1376, DF_ = 257;
constexpr int NT1 = F_ / 32;
constexpr int KS2 = F_ / 16;
constexpr float EPS = 1e-5f;

constexpr size_t ZSTR = 8192 + 256;
constexpr size_t OFF_Z = 0;
constexpr size_t OFF_W = OFF_Z + 256 * ZSTR;
constexpr size_t OFF_STD = OFF_W + 256 * ZSTR;
constexpr size_t OFF_MEAN = OFF_STD + 8192;
constexpr size_t OFF_W1F = OFF_MEAN + 8192;
constexpr size_t OFF_W2F = OFF_W1F + 60 * 32768;
constexpr size_t OFF_HT = OFF_W2F + 16 * KS2 * 1024;
constexpr size_t OFF_G = OFF_HT + 2097152;
constexpr size_t OFF_HRES = OFF_G + 64 * KS2 * 1024;
constexpr size_t OFF_END = OFF_HRES + 2097152;

DEV unsigned pk(float a, float b) {
  bf16x2 v; v.x = (__bf16)a; v.y = (__bf16)b;
  return __builtin_bit_cast(unsigned, v);
}
DEV float lo16(unsigned u) { return __builtin_bit_cast(float, u << 16); }
DEV float hi16(unsigned u) { return __builtin_bit_cast(float, u & 0xffff0000u); }
DEV float cosr(float rev) { return __builtin_amdgcn_cosf(rev); }
DEV float sinr(float rev) { return __builtin_amdgcn_sinf(rev); }
DEV f32x4 mma16(u32x4 a, u32x4 b, f32x4 c) {
  return __builtin_amdgcn_mfma_f32_16x16x32_bf16(__builtin_bit_cast(bf16x8, a), __builtin_bit_cast(bf16x8, b), c, 0, 0, 0);
}
DEV f32x16 mma32(u32x4 a, u32x4 b, f32x16 c) {
  return __builtin_amdgcn_mfma_f32_32x32x16_bf16(__builtin_bit_cast(bf16x8, a), __builtin_bit_cast(bf16x8, b), c, 0, 0, 0);
}
DEV u32x2 tr_read(LAS char* p) {
  return __builtin_bit_cast(u32x2, __builtin_amdgcn_ds_read_tr16_b64_v4i16((LAS s16x4*)p));
}
DEV float sq2(unsigned u, float acc) {
  const bf16x2 v = __builtin_bit_cast(bf16x2, u);
  return __builtin_amdgcn_fdot2_f32_bf16(v, v, acc, false);
}
DEV u32x4 neg4(u32x4 v) { return v ^ 0x80008000u; }
template <int CTRL> DEV float dpp_add(float v) {
  return v + __builtin_bit_cast(float, __builtin_amdgcn_update_dpp(0, __builtin_bit_cast(int, v), CTRL, 0xf, 0xf, true));
}
DEV float wave_sum(float v) {
  v = dpp_add<0xB1>(v);
  v = dpp_add<0x4E>(v);
  v = dpp_add<0x141>(v);
  v = dpp_add<0x140>(v);
  const int i = __builtin_bit_cast(int, v);
  return __builtin_bit_cast(float, __builtin_amdgcn_readlane(i, 0)) + __builtin_bit_cast(float, __builtin_amdgcn_readlane(i, 16)) +
         (__builtin_bit_cast(float, __builtin_amdgcn_readlane(i, 32)) + __builtin_bit_cast(float, __builtin_amdgcn_readlane(i, 48)));
}

struct RowDft {
  u32x4 c1[2], s1[2], a2;
  float twr[8], twi[8];
};
DEV void rowdft_init(RowDft& q, int l) {
  const int c = l & 15, g = l >> 4;
#pragma unroll
  for (int t = 0; t < 2; ++t) {
    const int j1 = 1 + 16 * t + c;
#pragma unroll
    for (int jj = 0; jj < 4; ++jj) {
      const int n1 = 4 * g + ((2 * jj) & 3) + 16 * (jj >> 1);
      const float r0 = (float)((n1 * j1) & 31) * (1.f / 32), r1 = (float)(((n1 + 1) * j1) & 31) * (1.f / 32);
      q.c1[t][jj] = pk(cosr(r0), cosr(r1));
      q.s1[t][jj] = pk(-sinr(r0), -sinr(r1));
    }
#pragma unroll
    for (int r = 0; r < 4; ++r) {
      const float rv = (float)((4 * g + r) * j1) * (1.f / 512);
      q.twr[4 * t + r] = cosr(rv) * (1.f / 16);
      q.twi[4 * t + r] = sinr(rv) * (-1.f / 16);
    }
  }
  const int j2 = c >> 1, p = c & 1;
#pragma unroll
  for (int jj = 0; jj < 4; ++jj) {
    const int n2 = 4 * g + ((2 * jj) & 3), hb = jj >> 1;
    const float sh = 0.25f * (float)(p - hb);
    q.a2[jj] = pk(cosr((float)((j2 * n2) & 15) * (1.f / 16) + sh), cosr((float)((j2 * (n2 + 1)) & 15) * (1.f / 16) + sh));
  }
}
DEV void rowdft(const RowDft& q, u32x4 a, int l, float (&o)[8]) {
  const f32x4 z = {0.f, 0.f, 0.f, 0.f};
#pragma unroll
  for (int t = 0; t < 2; ++t) {
    const f32x4 tr = mma16(a, q.c1[t], z), ti = mma16(a, q.s1[t], z);
    float ur[4], ui[4];
#pragma unroll
    for (int r = 0; r < 4; ++r) {
      ur[r] = tr[r] * q.twr[4 * t + r] - ti[r] * q.twi[4 * t + r];
      ui[r] = tr[r] * q.twi[4 * t + r] + ti[r] * q.twr[4 * t + r];
    }
    const u32x4 b = {pk(ur[0], ur[1]), pk(ur[2], ur[3]), pk(ui[0], ui[1]), pk(ui[2], ui[3])};
    const f32x4 x = mma16(q.a2, b, z);
#pragma unroll
    for (int r = 0; r < 4; ++r) o[4 * t + r] = x[r];
  }
  o[6] *= (l == 63) ? 0.70710678118654752f : 1.f;
  o[7] = (l == 63) ? 0.f : o[7];
}

DEV int m_of_f(int fl) { const int reg = 8 * (fl >> 4) + (fl & 7); return (reg & 3) + 8 * (reg >> 2) + 4 * ((fl >> 3) & 1); }
DEV int m_of_d(int dl) { const int reg = dl & 15; return (reg & 3) + 8 * (reg >> 2) + 4 * (dl >> 4); }
DEV int d_of_m(int m) { return 16 * ((m >> 2) & 1) + (m & 3) + 4 * (m >> 3); }

DEV void job_w1(int jb, const u16* __restrict__ w1, char* __restrict__ ws, LAS char* sm) {
  const int tid = threadIdx.x, l = tid & 63, w = tid >> 6;
  const int f0 = 8 * jb;
  const u32x4 v0 = *(const u32x4*)(w1 + (size_t)tid * F_ + f0);
  const u32x4 v1 = *(const u32x4*)(w1 + (size_t)(tid + 256) * F_ + f0);
  RowDft q;
  rowdft_init(q, l);
  *(LAS u32x4*)(sm + tid * 16) = v0;
  *(LAS u32x4*)(sm + 4096 + tid * 16) = v1;
  __syncthreads();
  const int c = l & 15, g = l >> 4;
  unsigned dw[8];
#pragma unroll
  for (int j = 0; j < 8; ++j) dw[j] = *(LAS unsigned*)(sm + (16 * (4 * g + (j & 3) + 16 * (j >> 2)) + c) * 16 + w * 4);
#pragma unroll
  for (int rr = 0; rr < 2; ++rr) {
    u32x4 a;
#pragma unroll
    for (int jj = 0; jj < 4; ++jj) a[jj] = __builtin_amdgcn_perm(dw[2 * jj + 1], dw[2 * jj], rr ? 0x07060302u : 0x05040100u);
    float o[8];
    rowdft(q, a, l, o);
    const int f = f0 + 2 * w + rr;
    const u32x4 r = {pk(o[0], o[1]), pk(o[2], o[3]), pk(o[4], o[5]), pk(o[6], o[7])};
    *(u32x4*)(ws + OFF_W1F + (size_t)(f >> 5) * 32768 + l * 512 + m_of_f(f & 31) * 16) = r;
  }
}
DEV void job_basis(int jb, char* __restrict__ ws) {
#pragma unroll
  for (int it = 0; it < 2; ++it) {
    const int v = (2 * jb + it) * 256 + threadIdx.x;
    const int dt = v >> 11, L = (v >> 5) & 63, m = v & 31;
    const int d = 32 * dt + d_of_m(m), k0 = 1 + (L & 15) + 64 * (L >> 4);
    u32x4 r;
#pragma unroll
    for (int e = 0; e < 4; ++e) {
      const int k = k0 + 16 * (e >> 1) + 32 * (e & 1);
      const float rv = (float)((k * d) & 511) * (1.f / 512);
      const float sc = (k == 256) ? 0.044194173824159220f : 0.0625f;
      r[e] = pk(cosr(rv) * sc, (k == 256) ? 0.f : sinr(rv) * -0.0625f);
    }
    *(u32x4*)(ws + OFF_W1F + (size_t)NT1 * 32768 + (size_t)v * 16) = r;
  }
}
DEV void job_w2(int jb, const u16* __restrict__ w2, char* __restrict__ ws) {
  const int l = threadIdx.x & 63, jw = 4 * jb + (threadIdx.x >> 6);
  u32x4 v[8];
#pragma unroll
  for (int j = 0; j < 8; ++j) v[j] = *(const u32x4*)(w2 + (size_t)(8 * jw + j) * D_ + 8 * l);
  char* dst = ws + OFF_W2F + ((size_t)(l >> 2) * KS2 + (jw >> 1)) * 1024 + (jw & 1) * 512;
#pragma unroll
  for (int i = 0; i < 8; ++i) {
    u32x4 r;
#pragma unroll
    for (int a = 0; a < 4; ++a) r[a] = __builtin_amdgcn_perm(v[2 * a + 1][i >> 1], v[2 * a][i >> 1], (i & 1) ? 0x07060302u : 0x05040100u);
    *(u32x4*)(dst + m_of_d(8 * (l & 3) + i) * 16) = r;
  }
}

constexpr int K1_W1 = 192, K1_BASIS = 64;
__global__ __launch_bounds__(256) void k1_dft(const u16* __restrict__ x, const u16* __restrict__ wr, const u16* __restrict__ wi,
                                              const u16* __restrict__ w1, char* __restrict__ ws) {
  __shared__ __attribute__((aligned(16))) char smem[16384];
  LAS char* sm = (LAS char*)smem;
  const int b = blockIdx.x, tid = threadIdx.x, l = tid & 63, w = tid >> 6;
  STAMP(0, 0);
  if (b >= 256) {
    const int jb = b - 256;
    if (jb < K1_W1) {
      const int j = (jb & 7) * 24 + (jb >> 3);
      if (j < F_ / 8) job_w1(j, w1, ws, sm);
    } else {
      job_basis(jb - K1_W1, ws);
    }
    STAMP(0, 3);
    return;
  }
  const int xcd = b & 7, i = b >> 3, pos = xcd * 32 + i;
  const int n2 = 8 * ((i & 15) >> 2) + 4 * (i >> 4) + (i & 3);
  const int n1 = 32 * (xcd >> 2) + 16 * (w >> 1) + 4 * (xcd & 3) + 2 * (w & 1);
  const int s0 = 32 * n1 + n2;
  const u32x4 xa = *(const u32x4*)(x + (size_t)s0 * D_ + 8 * l);
  const u32x4 xb = *(const u32x4*)(x + (size_t)(s0 + 32) * D_ + 8 * l);
  const int j0 = 16 * (pos >> 6) + (pos & 15) + 512 * ((pos >> 4) & 3);
  u16 fr[8], fi[8];
#pragma unroll
  for (int e = 0; e < 8; ++e) {
    fr[e] = wr[(size_t)(j0 + 64 * e) * DF_ + 1 + tid];
    fi[e] = wi[(size_t)(j0 + 64 * e) * DF_ + 1 + tid];
  }
  RowDft q;
  rowdft_init(q, l);
  LAS char* mine = sm + w * 2048;
  *(LAS u32x4*)(mine + l * 16) = xa;
  *(LAS u32x4*)(mine + 1024 + l * 16) = xb;
  STAMP(0, 1);
  float rs[2];
#pragma unroll
  for (int rr = 0; rr < 2; ++rr) {
    const u32x4 v = rr ? xb : xa;
    float f[8];
#pragma unroll
    for (int e = 0; e < 4; ++e) { f[2 * e] = lo16(v[e]); f[2 * e + 1] = hi16(v[e]); }
    const float mean = wave_sum(((f[0] + f[1]) + (f[2] + f[3])) + ((f[4] + f[5]) + (f[6] + f[7]))) * (1.f / D_);
    float ss = 0.f;
#pragma unroll
    for (int e = 0; e < 8; ++e) { const float dlt = f[e] - mean; ss += dlt * dlt; }
    const float var = wave_sum(ss) * (1.f / D_) + EPS;
    rs[rr] = __builtin_amdgcn_rsqf(var);
    if (l == 0) {
      ((float*)(ws + OFF_STD))[s0 + 32 * rr] = var * rs[rr];
      ((float*)(ws + OFF_MEAN))[s0 + 32 * rr] = mean;
    }
  }
  float o[2][8];
#pragma unroll
  for (int rr = 0; rr < 2; ++rr) {
    const u32x2 a0 = tr_read(mine + rr * 1024 + l * 8), a1 = tr_read(mine + rr * 1024 + 512 + l * 8);
    const u32x4 a = {a0[0], a0[1], a1[0], a1[1]};
    rowdft(q, a, l, o[rr]);
  }
#pragma unroll
  for (int e = 0; e < 8; ++e) *(LAS unsigned*)(sm + 8192 + (e * 64 + l) * 16 + w * 4) = pk(o[0][e] * rs[0], o[1][e] * rs[1]);
  {
    u32x4 p0, p1;
#pragma unroll
    for (int e = 0; e < 4; ++e) {
      p0[e] = (unsigned)fr[e] | ((unsigned)fi[e] << 16);
      p1[e] = (unsigned)fr[e + 4] | ((unsigned)fi[e + 4] << 16);
    }
    char* dst = ws + OFF_W + (size_t)tid * ZSTR + pos * 32;
    *(u32x4*)dst = p0;
    *(u32x4*)(dst + 16) = p1;
  }
  __syncthreads();
  STAMP(0, 2);
#pragma unroll
  for (int it = 0; it < 2; ++it) {
    const int kk = (tid >> 1) + 128 * it, p = tid & 1;
    const int v = (4 * ((kk >> 4) & 1) + 2 * ((kk >> 5) & 1) + p) * 64 + (kk & 15) + 16 * (kk >> 6);
    *(u32x4*)(ws + OFF_Z + (size_t)kk * ZSTR + pos * 32 + p * 16) = *(LAS u32x4*)(sm + 8192 + v * 16);
  }
  STAMP(0, 3);
}

constexpr int K2_W2 = 48;
__global__ __launch_bounds__(256) void k2_col(const u16* __restrict__ w2, char* __restrict__ ws) {
  __shared__ __attribute__((aligned(16))) char smem[8192];
  LAS char* sm = (LAS char*)smem;
  const int kk = blockIdx.x, tid = threadIdx.x, l = tid & 63, w = tid >> 6;
  STAMP(1, 0);
  if (kk >= 256) {
    if (kk - 256 < F_ / 32) job_w2(kk - 256, w2, ws);
    STAMP(1, 3);
    return;
  }
  const int c = l & 15, g = l >> 4, kw = w >> 1;
  u32x4 zr[2][2], zi[2][2];
#pragma unroll
  for (int ks = 0; ks < 2; ++ks)
#pragma unroll
    for (int t = 0; t < 2; ++t) {
      const char* src = ws + OFF_Z + (size_t)kk * ZSTR + ((((ks ^ kw) * 4 + g) * 32) + 16 * t + c) * 32;
      zr[t][ks] = *(const u32x4*)src;
      zi[t][ks] = *(const u32x4*)(src + 16);
    }
  const u32x4 wf0 = *(const u32x4*)(ws + OFF_W + (size_t)kk * ZSTR + tid * 32);
  const u32x4 wf1 = *(const u32x4*)(ws + OFF_W + (size_t)kk * ZSTR + tid * 32 + 16);
  const int jc = 16 * w + c;
  const int sb = 32 * jc + 8 * g;
  f32x4 sd[2];
  sd[0] = *(const f32x4*)(ws + OFF_STD + sb * 4);
  sd[1] = *(const f32x4*)(ws + OFF_STD + sb * 4 + 16);

  u32x4 c1[2], s1[2], c2[2], s2[2], sel;
  float t1r[8], t1i[8], t2r[8], t2i[8];
#pragma unroll
  for (int ks = 0; ks < 2; ++ks)
#pragma unroll
    for (int jj = 0; jj < 4; ++jj) {
      const int n = 32 * (ks ^ kw) + 16 * (jj >> 1) + 4 * g + ((2 * jj) & 3);
      const float r0 = (float)((n * jc) & 63) * (1.f / 64), r1 = (float)(((n + 1) * jc) & 63) * (1.f / 64);
      c1[ks][jj] = pk(cosr(r0), cosr(r1));
      s1[ks][jj] = pk(sinr(r0), sinr(r1));
    }
#pragma unroll
  for (int t = 0; t < 2; ++t) {
    const int sg = 8 * (c >> 2) + 4 * t + (c & 3);
#pragma unroll
    for (int jj = 0; jj < 4; ++jj) {
      const int n = 8 * g + 2 * jj;
      const float r0 = (float)((n * sg) & 31) * (1.f / 32), r1 = (float)(((n + 1) * sg) & 31) * (1.f / 32);
      c2[t][jj] = pk(cosr(r0), cosr(r1));
      s2[t][jj] = pk(sinr(r0), sinr(r1));
    }
#pragma unroll
    for (int r = 0; r < 4; ++r) {
      const float a = (float)((8 * g + 4 * t + r) * jc) * (1.f / 2048);
      t1r[4 * t + r] = cosr(a);
      t1i[4 * t + r] = -sinr(a);
      const float bq = (float)(sg * (16 * w + 4 * g + r)) * (1.f / 2048);
      t2r[4 * t + r] = cosr(bq) * (1.f / 2048);
      t2i[4 * t + r] = sinr(bq) * (1.f / 2048);
    }
  }
#pragma unroll
  for (int jj = 0; jj < 4; ++jj) {
    const bool on = ((jj >> 1) == (w & 1)) && ((c >> 2) == g) && (((c & 3) >> 1) == (jj & 1));
    sel[jj] = on ? ((c & 1) ? 0x3f800000u : 0x00003f80u) : 0u;
  }
  const f32x4 z4 = {0.f, 0.f, 0.f, 0.f};
  STAMP(1, 1);
  u32x4 bre, bim;
  f32x4 hr[2], hi[2];
#pragma unroll
  for (int t = 0; t < 2; ++t) {
    f32x4 tr = mma16(zr[t][0], c1[0], z4);
    f32x4 ti = mma16(zi[t][0], c1[0], z4);
    tr = mma16(zi[t][0], s1[0], tr);
    ti = mma16(zr[t][0], neg4(s1[0]), ti);
    tr = mma16(zr[t][1], c1[1], tr);
    ti = mma16(zi[t][1], c1[1], ti);
    tr = mma16(zi[t][1], s1[1], tr);
    ti = mma16(zr[t][1], neg4(s1[1]), ti);
    float ur[4], ui[4];
#pragma unroll
    for (int r = 0; r < 4; ++r) {
      ur[r] = tr[r] * t1r[4 * t + r] - ti[r] * t1i[4 * t + r];
      ui[r] = tr[r] * t1i[4 * t + r] + ti[r] * t1r[4 * t + r];
    }
    bre[2 * t] = pk(ur[0], ur[1]); bre[2 * t + 1] = pk(ur[2], ur[3]);
    bim[2 * t] = pk(ui[0], ui[1]); bim[2 * t + 1] = pk(ui[2], ui[3]);
    hr[t] = mma16(zr[t][0], sel, z4) * sd[t];
    hi[t] = mma16(zi[t][0], sel, z4) * sd[t];
  }
  u32x4 are, aim;
#pragma unroll
  for (int t = 0; t < 2; ++t) {
    f32x4 xr = mma16(c2[t], bre, z4);
    f32x4 xi = mma16(c2[t], bim, z4);
    xr = mma16(s2[t], bim, xr);
    xi = mma16(neg4(s2[t]), bre, xi);
    const u32x4 wf = t ? wf1 : wf0;
    float ur[4], ui[4];
#pragma unroll
    for (int r = 0; r < 4; ++r) {
      const float a = lo16(wf[r]), bq = hi16(wf[r]);
      ur[r] = xr[r] * a - xi[r] * bq;
      ui[r] = xr[r] * bq + xi[r] * a;
    }
    are[2 * t] = pk(ur[0], ur[1]); are[2 * t + 1] = pk(ur[2], ur[3]);
    aim[2 * t] = pk(ui[0], ui[1]); aim[2 * t + 1] = pk(ui[2], ui[3]);
  }
#pragma unroll
  for (int t = 0; t < 2; ++t) {
    f32x4 vr = mma16(are, c2[t], z4);
    f32x4 vi = mma16(aim, c2[t], z4);
    vr = mma16(aim, neg4(s2[t]), vr);
    vi = mma16(are, s2[t], vi);
    float ur[4], ui[4];
#pragma unroll
    for (int r = 0; r < 4; ++r) {
      ur[r] = vr[r] * t2r[4 * t + r] - vi[r] * t2i[4 * t + r];
      ui[r] = vr[r] * t2i[4 * t + r] + vi[r] * t2r[4 * t + r];
    }
    const u32x4 f = {pk(ur[0], ur[1]), pk(ur[2], ur[3]), pk(ui[0], ui[1]), pk(ui[2], ui[3])};
    *(LAS u32x4*)(sm + (w * 2 + t) * 1024 + l * 16) = f;
  }
  __syncthreads();
  STAMP(1, 2);
#pragma unroll
  for (int ks = 0; ks < 2; ++ks)
#pragma unroll
    for (int hh = 0; hh < 2; ++hh) {
      const int src = 2 * (ks ^ kw) + hh;
      const u32x4 br = {c1[ks][2 * hh], c1[ks][2 * hh + 1], s1[ks][2 * hh] ^ 0x80008000u, s1[ks][2 * hh + 1] ^ 0x80008000u};
      const u32x4 bi = {s1[ks][2 * hh], s1[ks][2 * hh + 1], c1[ks][2 * hh], c1[ks][2 * hh + 1]};
#pragma unroll
      for (int t = 0; t < 2; ++t) {
        const u32x4 u = *(LAS u32x4*)(sm + (src * 2 + t) * 1024 + l * 16);
        hr[t] = mma16(u, br, hr[t]);
        hi[t] = mma16(u, bi, hi[t]);
      }
    }
  const int kp = 8 * ((kk & 15) + 16 * (kk >> 6)) + 4 * ((kk >> 4) & 1) + 2 * ((kk >> 5) & 1);
  char* dst = ws + OFF_HT + (size_t)(sb >> 7) * 131072 + kp * 256 + (sb & 127) * 2;
  const u32x4 ore = {pk(hr[0][0], hr[0][1]), pk(hr[0][2], hr[0][3]), pk(hr[1][0], hr[1][1]), pk(hr[1][2], hr[1][3])};
  u32x4 oim = {pk(hi[0][0], hi[0][1]), pk(hi[0][2], hi[0][3]), pk(hi[1][0], hi[1][1]), pk(hi[1][2], hi[1][3])};
  if (kk == 255) oim = u32x4{0u, 0u, 0u, 0u};
  *(u32x4*)dst = ore;
  *(u32x4*)(dst + 256) = oim;
  STAMP(1, 3);
}

#define GLDS(voff, sbase, ldsdst)                                                                                     \
  { unsigned keep_;                                                                                                   \
    asm volatile("s_mov_b32 %0, m0\n\ts_mov_b32 m0, %3\n\ts_nop 0\n\tglobal_load_lds_dwordx4 %1, %2\n\ts_mov_b32 m0, %0" \
                 : "=&s"(keep_) : "v"(voff), "s"(sbase), "s"(ldsdst) : "memory"); }
#define GLOAD(dst, voff, sbase, OFF) \
  asm volatile("global_load_dwordx4 %0, %1, %2 offset:%3" : "=&v"(dst) : "v"(voff), "s"(sbase), "i"(OFF) : "memory")

template <int C> struct K3Chunk {
  static DEV void issue(u32x4 (&bw)[32], unsigned va, unsigned vb, const char* ht, const char* wt, unsigned ldsw) {
#pragma unroll
    for (int u = 0; u < 4; ++u) {
      const char* sa = ht + C * 16384 + u * 1024;
      const unsigned ld = ldsw + C * 16384 + u * 1024;
      GLDS(va, sa, ld);
    }
    const char* sw = wt + C * 4096;
    GLOAD(bw[4 * C + 0], vb, sw, 0);
    GLOAD(bw[4 * C + 1], vb, sw, 1024);
    GLOAD(bw[4 * C + 2], vb, sw, 2048);
    GLOAD(bw[4 * C + 3], vb, sw, 3072);
  }
  static DEV void compute(u32x4 (&bw)[32], LAS char* const (&rd)[4], f32x16 (&acc)[4], float& ssq) {
    asm volatile("s_waitcnt vmcnt(%4)\n\ts_barrier"
                 : "+v"(bw[4 * C]), "+v"(bw[4 * C + 1]), "+v"(bw[4 * C + 2]), "+v"(bw[4 * C + 3]) : "i"(C == 0 ? 48 : 8 * (7 - C)) : "memory");
#pragma unroll
    for (int u = 0; u < 4; ++u) {
      const int ks = 4 * C + u;
#pragma unroll
      for (int t = 0; t < 4; ++t) {
        const u32x2 a0 = tr_read(rd[t] + ks * 4096), a1 = tr_read(rd[t] + ks * 4096 + 1024);
        const u32x4 hf = {a0[0], a0[1], a1[0], a1[1]};
        acc[t] = mma32(bw[ks], hf, acc[t]);
        if (t == 0) {
#pragma unroll
          for (int e = 0; e < 4; ++e) ssq = sq2(hf[e], ssq);
        }
      }
    }
  }
};

__global__ __launch_bounds__(256, 1) void k3_gemm1(const u16* __restrict__ b1, char* __restrict__ ws) {
  __shared__ __attribute__((aligned(16))) char smem[131072 + 1024];
  LAS char* sm = (LAS char*)smem;
  const int b = blockIdx.x, tid = threadIdx.x, l = tid & 63;
  const int w = __builtin_amdgcn_readfirstlane(tid >> 6);
  const int xcd = b & 7, i = b >> 3;
  STAMP(2, 0);
  if (i >= 30) return;
  const int sblk = 2 * xcd + (i & 1), nt = 4 * (i >> 1) + w, ntl = nt < 58 ? nt : 58;
  const int n = l & 31, h = l >> 5;
  u32x4 e0, e1;
  {
    const char* pb = (const char*)(b1 + 32 * (ntl < NT1 ? ntl : 0));
    const unsigned vo = h * 16;
    GLOAD(e0, vo, pb, 0);
    GLOAD(e1, vo, pb, 32);
  }
  float mu[4];
#pragma unroll
  for (int t = 0; t < 4; ++t) {
    const char* pm = ws + OFF_MEAN + (128 * sblk + 32 * ((t + w) & 3)) * 4;
    const unsigned vo = n * 4;
    asm volatile("global_load_dword %0, %1, %2" : "=&v"(mu[t]) : "v"(vo), "s"(pm) : "memory");
  }
  const char* ht = ws + OFF_HT + (size_t)sblk * 131072 + w * 4096;
  const char* wt = ws + OFF_W1F + (size_t)ntl * 32768;
  const unsigned va = (l >> 4) * 256 + (((l & 15) ^ ((l >> 4) << 2)) * 16), vb = l * 16;
  const unsigned ldsw = (unsigned)(uintptr_t)sm + w * 4096;
  u32x4 bw[32];
  K3Chunk<0>::issue(bw, va, vb, ht, wt, ldsw);
  K3Chunk<1>::issue(bw, va, vb, ht, wt, ldsw);
  K3Chunk<2>::issue(bw, va, vb, ht, wt, ldsw);
  K3Chunk<3>::issue(bw, va, vb, ht, wt, ldsw);
  K3Chunk<4>::issue(bw, va, vb, ht, wt, ldsw);
  K3Chunk<5>::issue(bw, va, vb, ht, wt, ldsw);
  K3Chunk<6>::issue(bw, va, vb, ht, wt, ldsw);
  const int q = (l & 15) >> 2;
  LAS char* const rb = sm + (8 * h + q) * 256 + ((l >> 4) & 1) * 32 + (l & 3) * 8;
  LAS char* const rd[4] = {rb + (((0 + w) & 3) ^ q) * 64, rb + (((1 + w) & 3) ^ q) * 64, rb + (((2 + w) & 3) ^ q) * 64, rb + (((3 + w) & 3) ^ q) * 64};
  f32x16 acc[4];
#pragma unroll
  for (int t = 0; t < 4; ++t)
#pragma unroll
    for (int r = 0; r < 16; ++r) acc[t][r] = 0.f;
  float ssq = 0.f;
  asm volatile("s_waitcnt vmcnt(48)" : "+v"(e0), "+v"(e1), "+v"(mu[0]), "+v"(mu[1]), "+v"(mu[2]), "+v"(mu[3]) : : "memory");
  STAMP(2, 1);
  K3Chunk<0>::compute(bw, rd, acc, ssq);
  K3Chunk<7>::issue(bw, va, vb, ht, wt, ldsw);
  K3Chunk<1>::compute(bw, rd, acc, ssq);
  K3Chunk<2>::compute(bw, rd, acc, ssq);
  K3Chunk<3>::compute(bw, rd, acc, ssq);
  K3Chunk<4>::compute(bw, rd, acc, ssq);
  K3Chunk<5>::compute(bw, rd, acc, ssq);
  K3Chunk<6>::compute(bw, rd, acc, ssq);
  K3Chunk<7>::compute(bw, rd, acc, ssq);
  *(LAS float*)(sm + 131072 + tid * 4) = ssq;
  __syncthreads();
  STAMP(2, 2);
  if (nt < NT1) {
    float bias[16];
#pragma unroll
    for (int e = 0; e < 4; ++e) {
      bias[2 * e] = lo16(e0[e]); bias[2 * e + 1] = hi16(e0[e]);
      bias[8 + 2 * e] = lo16(e1[e]); bias[8 + 2 * e + 1] = hi16(e1[e]);
    }
#pragma unroll
    for (int t = 0; t < 4; ++t) {
      const int ta = (t + w) & 3;
      const float tot = *(LAS float*)(sm + 131072 + (ta * 64 + n) * 4) + *(LAS float*)(sm + 131072 + (ta * 64 + 32 + n) * 4);
      const float rstd = __builtin_amdgcn_rsqf(tot * (1.f / D_) + EPS);
      float gv[16];
#pragma unroll
      for (int r = 0; r < 16; ++r) {
        const float pre = acc[t][r] * rstd + bias[r];
        const float p = pre > 0.f ? pre : 0.f;
        gv[r] = p * p;
      }
      char* dst = ws + OFF_G + ((size_t)(4 * sblk + ta) * KS2 + 2 * nt) * 1024 + l * 16;
      *(u32x4*)dst = u32x4{pk(gv[0], gv[1]), pk(gv[2], gv[3]), pk(gv[4], gv[5]), pk(gv[6], gv[7])};
      *(u32x4*)(dst + 1024) = u32x4{pk(gv[8], gv[9]), pk(gv[10], gv[11]), pk(gv[12], gv[13]), pk(gv[14], gv[15])};
    }
  } else if (nt < NT1 + 16) {
#pragma unroll
    for (int t = 0; t < 4; ++t) {
      const int ta = (t + w) & 3;
      float gv[16];
#pragma unroll
      for (int r = 0; r < 16; ++r) gv[r] = acc[t][r] + mu[t];
      char* dst = ws + OFF_HRES + ((size_t)(4 * sblk + ta) * 16 + (nt - NT1)) * 2048 + l * 16;
      *(u32x4*)dst = u32x4{pk(gv[0], gv[1]), pk(gv[2], gv[3]), pk(gv[4], gv[5]), pk(gv[6], gv[7])};
      *(u32x4*)(dst + 1024) = u32x4{pk(gv[8], gv[9]), pk(gv[10], gv[11]), pk(gv[12], gv[13]), pk(gv[14], gv[15])};
    }
  }
  STAMP(2, 3);
}

constexpr int K4_DEPTH = 8;
__global__ __launch_bounds__(256, 1) void k4_gemm2(const u16* __restrict__ b2, const char* __restrict__ ws, u16* __restrict__ out) {
  __shared__ __attribute__((aligned(16))) char smem[49152];
  LAS char* sm = (LAS char*)smem;
  const int b = blockIdx.x, tid = threadIdx.x, l = tid & 63;
  const int w = __builtin_amdgcn_readfirstlane(tid >> 6);
  const int xcd = b & 7, i = b >> 3;
  STAMP(3, 0);
  const int st0 = 2 * (4 * xcd + (i & 3)), dt0 = 2 * (i >> 2);
  const int k0 = w < 2 ? 22 * w : 21 * w + 2;
  const int sa = st0 + (w >> 1), da = dt0 + (w & 1);
  const char* g0 = ws + OFF_G + ((size_t)sa * KS2 + k0) * 1024 + l * 16;
  const char* g1 = ws + OFF_G + ((size_t)(sa ^ 1) * KS2 + k0) * 1024 + l * 16;
  const char* v0 = ws + OFF_W2F + ((size_t)da * KS2 + k0) * 1024 + l * 16;
  const char* v1 = ws + OFF_W2F + ((size_t)(da ^ 1) * KS2 + k0) * 1024 + l * 16;
  u32x4 fg0[22], fg1[22], fv0[22], fv1[22];
#pragma unroll
  for (int k = 0; k < K4_DEPTH; ++k) {
    fg0[k] = *(const u32x4*)(g0 + k * 1024); fv0[k] = *(const u32x4*)(v0 + k * 1024);
    fg1[k] = *(const u32x4*)(g1 + k * 1024); fv1[k] = *(const u32x4*)(v1 + k * 1024);
  }
  const int n = l & 31, h = l >> 5;
  f32x16 acc[4];
#pragma unroll
  for (int t = 0; t < 4; ++t)
#pragma unroll
    for (int r = 0; r < 16; ++r) acc[t][r] = 0.f;
  u32x4 r0, r1, e0, e1;
  __builtin_amdgcn_sched_barrier(0);
#pragma unroll
  for (int k = 0; k < 21; ++k) {
    const int kn = k + K4_DEPTH;
    if (kn < 21) {
      fg0[kn] = *(const u32x4*)(g0 + kn * 1024); fv0[kn] = *(const u32x4*)(v0 + kn * 1024);
      fg1[kn] = *(const u32x4*)(g1 + kn * 1024); fv1[kn] = *(const u32x4*)(v1 + kn * 1024);
    } else if (kn == 21) {
      const int o = w < 2 ? 21 * 1024 : 20 * 1024;
      fg0[kn] = *(const u32x4*)(g0 + o); fv0[kn] = *(const u32x4*)(v0 + o);
      fg1[kn] = *(const u32x4*)(g1 + o); fv1[kn] = *(const u32x4*)(v1 + o);
    } else if (kn == 22) {
      const char* pr = ws + OFF_HRES + ((size_t)sa * 16 + da) * 2048 + l * 16;
      r0 = *(const u32x4*)pr; r1 = *(const u32x4*)(pr + 1024);
      const u16* pb = b2 + 32 * da + 16 * h;
      e0 = *(const u32x4*)pb; e1 = *(const u32x4*)(pb + 8);
    }
    __builtin_amdgcn_sched_barrier(0);
    acc[0] = mma32(fv0[k], fg0[k], acc[0]);
    acc[2] = mma32(fv0[k], fg1[k], acc[2]);
    acc[1] = mma32(fv1[k], fg0[k], acc[1]);
    acc[3] = mma32(fv1[k], fg1[k], acc[3]);
    __builtin_amdgcn_sched_barrier(0);
    if (k == 0) { STAMP(3, 1); }
  }
  if (w < 2) {
    acc[0] = mma32(fv0[21], fg0[21], acc[0]);
    acc[2] = mma32(fv0[21], fg1[21], acc[2]);
    acc[1] = mma32(fv1[21], fg0[21], acc[1]);
    acc[3] = mma32(fv1[21], fg1[21], acc[3]);
  }
#pragma unroll
  for (int j = 1; j < 4; ++j)
#pragma unroll
    for (int qd = 0; qd < 4; ++qd)
      *(LAS f32x4*)(sm + (((w ^ j) * 3 + (j - 1)) * 4 + qd) * 1024 + l * 16) =
          f32x4{acc[j][4 * qd], acc[j][4 * qd + 1], acc[j][4 * qd + 2], acc[j][4 * qd + 3]};
  __syncthreads();
  STAMP(3, 2);
  float o[16];
#pragma unroll
  for (int qd = 0; qd < 4; ++qd) {
    const f32x4 p1 = *(LAS f32x4*)(sm + ((w * 3 + 0) * 4 + qd) * 1024 + l * 16);
    const f32x4 p2 = *(LAS f32x4*)(sm + ((w * 3 + 1) * 4 + qd) * 1024 + l * 16);
    const f32x4 p3 = *(LAS f32x4*)(sm + ((w * 3 + 2) * 4 + qd) * 1024 + l * 16);
#pragma unroll
    for (int r = 0; r < 4; ++r) o[4 * qd + r] = (acc[0][4 * qd + r] + p1[r]) + (p2[r] + p3[r]);
  }
#pragma unroll
  for (int e = 0; e < 4; ++e) {
    o[2 * e] += lo16(r0[e]) + lo16(e0[e]); o[2 * e + 1] += hi16(r0[e]) + hi16(e0[e]);
    o[8 + 2 * e] += lo16(r1[e]) + lo16(e1[e]); o[8 + 2 * e + 1] += hi16(r1[e]) + hi16(e1[e]);
  }
  u16* dst = out + (size_t)(32 * sa + n) * D_ + 32 * da + 16 * h;
  *(u32x4*)dst = u32x4{pk(o[0], o[1]), pk(o[2], o[3]), pk(o[4], o[5]), pk(o[6], o[7])};
  *(u32x4*)(dst + 8) = u32x4{pk(o[8], o[9]), pk(o[10], o[11]), pk(o[12], o[13]), pk(o[14], o[15])};
  STAMP(3, 3);
}

extern "C" void kernel_launch(void* const* d_in, const int* in_sizes, int n_in, void* d_out, int out_size, void* d_ws,
                              size_t ws_size, hipStream_t stream) {
  const u16* x = (const u16*)d_in[0];
  const u16* wr = (const u16*)d_in[1];
  const u16* wi = (const u16*)d_in[2];
  const u16* w1 = (const u16*)d_in[4];
  const u16* b1 = (const u16*)d_in[5];
  const u16* w2 = (const u16*)d_in[6];
  const u16* b2 = (const u16*)d_in[7];
  char* ws = (char*)d_ws;
  k1_dft<<<256 + K1_W1 + K1_BASIS, 256, 0, stream>>>(x, wr, wi, w1, ws);
  k2_col<<<256 + K2_W2, 256, 0, stream>>>(w2, ws);
  k3_gemm1<<<256, 256, 0, stream>>>(b1, ws);
  k4_gemm2<<<256, 256, 0, stream>>>(b2, ws, (u16*)d_out);
}
```

```cpp
#include <hip/hip_runtime.h>
#include <hip/hip_bf16.h>
#include <stdint.h>

typedef __attribute__((ext_vector_type(8))) __bf16 bf16x8;
typedef __attribute__((ext_vector_type(2))) __bf16 bf16x2;
typedef __attribute__((ext_vector_type(4))) short s16x4;
typedef __attribute__((ext_vector_type(4))) float f32x4;
typedef __attribute__((ext_vector_type(16))) float f32x16;
typedef __attribute__((ext_vector_type(4))) unsigned u32x4;
typedef __attribute__((ext_vector_type(2))) unsigned u32x2;
typedef unsigned short u16;
#define LAS __attribute__((address_space(3)))
#define DEV __device__ __forceinline__

typedef __attribute__((ext_vector_type(2))) float f32x2;
#define STAMP_DECL
#define STAMP(i)
#define STAMP_FLUSH

constexpr int S_ = 2048, D_ = 512, F_ = 1376, DF_ = 257;
constexpr int KS2 = F_ / 16;
constexpr float EPS = 1e-5f;
constexpr int SPIN = 1 << 16;

constexpr unsigned ZSTR = 16384 + 256, WSTR = 12288 + 256;
constexpr unsigned OFF_E = 0;
constexpr unsigned OFF_AUX = 1024;
constexpr unsigned OFF_F3 = 16384;
constexpr unsigned OFF_MEAN = OFF_F3 + 32768;
constexpr unsigned OFF_Z = 65536;
constexpr unsigned OFF_W = OFF_Z + 256 * ZSTR;
constexpr unsigned OFF_H = OFF_W + 256 * WSTR;
constexpr unsigned OFF_W1F = OFF_H + 2097152;
constexpr unsigned OFF_W2F = OFF_W1F + KS2 * 16384;
constexpr unsigned OFF_G = OFF_W2F + 16 * KS2 * 1024;
constexpr unsigned OFF_END = OFF_G + 32 * KS2 * 2048;

constexpr int L_H = 0, L_BAS = 65536, L_TMP = 131072, L_END = 163840;

DEV unsigned pk(float a, float b) {
  bf16x2 v; v.x = (__bf16)a; v.y = (__bf16)b;
  return __builtin_bit_cast(unsigned, v);
}
DEV float lo16(unsigned u) { return __builtin_bit_cast(float, u << 16); }
DEV float hi16(unsigned u) { return __builtin_bit_cast(float, u & 0xffff0000u); }
DEV float cosr(float rev) { return __builtin_amdgcn_cosf(rev); }
DEV float sinr(float rev) { return __builtin_amdgcn_sinf(rev); }
DEV f32x4 mma16(u32x4 a, u32x4 b, f32x4 c) {
  return __builtin_amdgcn_mfma_f32_16x16x32_bf16(__builtin_bit_cast(bf16x8, a), __builtin_bit_cast(bf16x8, b), c, 0, 0, 0);
}
DEV f32x16 mma32(u32x4 a, u32x4 b, f32x16 c) {
  return __builtin_amdgcn_mfma_f32_32x32x16_bf16(__builtin_bit_cast(bf16x8, a), __builtin_bit_cast(bf16x8, b), c, 0, 0, 0);
}
DEV u32x2 tr_read(LAS char* p) {
  return __builtin_bit_cast(u32x2, __builtin_amdgcn_ds_read_tr16_b64_v4i16((LAS s16x4*)p));
}
DEV float sq2(unsigned u, float acc) {
  const bf16x2 v = __builtin_bit_cast(bf16x2, u);
  return __builtin_amdgcn_fdot2_f32_bf16(v, v, acc, false);
}
DEV u32x4 neg4(u32x4 v) { return v ^ 0x80008000u; }
template <int CTRL> DEV float dpp_add(float v) {
  return v + __builtin_bit_cast(float, __builtin_amdgcn_update_dpp(0, __builtin_bit_cast(int, v), CTRL, 0xf, 0xf, true));
}
DEV float wave_sum(float v) {
  v = dpp_add<0xB1>(v);
  v = dpp_add<0x4E>(v);
  v = dpp_add<0x141>(v);
  v = dpp_add<0x140>(v);
  const int i = __builtin_bit_cast(int, v);
  return __builtin_bit_cast(float, __builtin_amdgcn_readlane(i, 0)) + __builtin_bit_cast(float, __builtin_amdgcn_readlane(i, 16)) +
         (__builtin_bit_cast(float, __builtin_amdgcn_readlane(i, 32)) + __builtin_bit_cast(float, __builtin_amdgcn_readlane(i, 48)));
}

struct RowDft {
  u32x4 c1[2], s1[2], a2;
  float twr[8], twi[8];
};
DEV void rowdft_init(RowDft& q, int l) {
  const int c = l & 15, g = l >> 4;
#pragma unroll
  for (int t = 0; t < 2; ++t) {
    const int j1 = 1 + 16 * t + c;
#pragma unroll
    for (int jj = 0; jj < 4; ++jj) {
      const int n1 = 4 * g + ((2 * jj) & 3) + 16 * (jj >> 1);
      const float r0 = (float)((n1 * j1) & 31) * (1.f / 32), r1 = (float)(((n1 + 1) * j1) & 31) * (1.f / 32);
      q.c1[t][jj] = pk(cosr(r0), cosr(r1));
      q.s1[t][jj] = pk(-sinr(r0), -sinr(r1));
    }
#pragma unroll
    for (int r = 0; r < 4; ++r) {
      const float rv = (float)((4 * g + r) * j1) * (1.f / 512);
      q.twr[4 * t + r] = cosr(rv) * (1.f / 16);
      q.twi[4 * t + r] = sinr(rv) * (-1.f / 16);
    }
  }
  const int j2 = c >> 1, p = c & 1;
#pragma unroll
  for (int jj = 0; jj < 4; ++jj) {
    const int n2 = 4 * g + ((2 * jj) & 3), hb = jj >> 1;
    const float sh = 0.25f * (float)(p - hb);
    q.a2[jj] = pk(cosr((float)((j2 * n2) & 15) * (1.f / 16) + sh), cosr((float)((j2 * (n2 + 1)) & 15) * (1.f / 16) + sh));
  }
}
DEV void rowdft(const RowDft& q, u32x4 a, int l, float (&o)[8]) {
  const f32x4 z = {0.f, 0.f, 0.f, 0.f};
#pragma unroll
  for (int t = 0; t < 2; ++t) {
    const f32x4 tr = mma16(a, q.c1[t], z), ti = mma16(a, q.s1[t], z);
    float ur[4], ui[4];
#pragma unroll
    for (int r = 0; r < 4; ++r) {
      ur[r] = tr[r] * q.twr[4 * t + r] - ti[r] * q.twi[4 * t + r];
      ui[r] = tr[r] * q.twi[4 * t + r] + ti[r] * q.twr[4 * t + r];
    }
    const u32x4 b = {pk(ur[0], ur[1]), pk(ur[2], ur[3]), pk(ui[0], ui[1]), pk(ui[2], ui[3])};
    const f32x4 x = mma16(q.a2, b, z);
#pragma unroll
    for (int r = 0; r < 4; ++r) o[4 * t + r] = x[r];
  }
  o[6] *= (l == 63) ? 0.70710678118654752f : 1.f;
  o[7] = (l == 63) ? 0.f : o[7];
}

DEV int d_of_m(int m) { return 16 * ((m >> 2) & 1) + (m & 3) + 4 * (m >> 3); }

typedef __amdgpu_buffer_rsrc_t rsrc_t;
DEV u32x4 ld4(rsrc_t r, unsigned v, unsigned s) { return __builtin_amdgcn_raw_buffer_load_b128(r, v, s, 16); }
DEV unsigned ld1(rsrc_t r, unsigned v, unsigned s) { return __builtin_amdgcn_raw_buffer_load_b32(r, v, s, 16); }
DEV void st4(u32x4 d, rsrc_t r, unsigned v, unsigned s) { __builtin_amdgcn_raw_buffer_store_b128(d, r, v, s, 16); }
DEV void st1(unsigned d, rsrc_t r, unsigned v, unsigned s) { __builtin_amdgcn_raw_buffer_store_b32(d, r, v, s, 16); }
DEV void drain() { asm volatile("s_waitcnt vmcnt(0)" ::: "memory"); }
DEV void bar() { asm volatile("s_waitcnt lgkmcnt(0)\n\ts_barrier" ::: "memory"); }
DEV void fence() { asm volatile("" ::: "memory"); __builtin_amdgcn_sched_barrier(0); }
DEV bool any(bool p) { return __builtin_amdgcn_ballot_w64(p) != 0; }
DEV unsigned umin(unsigned a, unsigned b) { return a < b ? a : b; }
DEV unsigned umax(unsigned a, unsigned b) { return a > b ? a : b; }
DEV u32x4 clean(u32x4 v) {
  return u32x4{umin(v[0], 0xfffffffeu), umin(v[1], 0xfffffffeu), umin(v[2], 0xfffffffeu), umin(v[3], 0xfffffffeu)};
}
DEV float xsum(float v) {
  const unsigned u = __builtin_bit_cast(unsigned, v);
  const auto a = __builtin_amdgcn_permlane16_swap(u, u, false, false);
  const unsigned a0 = a[0], a1 = a[1];
  const float s = __builtin_bit_cast(float, a0) + __builtin_bit_cast(float, a1);
  const unsigned su = __builtin_bit_cast(unsigned, s);
  const auto q = __builtin_amdgcn_permlane32_swap(su, su, false, false);
  const unsigned q0 = q[0], q1 = q[1];
  return __builtin_bit_cast(float, q0) + __builtin_bit_cast(float, q1);
}

__global__ __launch_bounds__(256, 1) void fused(const u16* __restrict__ x, const u16* __restrict__ wr, const u16* __restrict__ wi,
                                                const u16* __restrict__ w1, const u16* __restrict__ b1, const u16* __restrict__ w2,
                                                const u16* __restrict__ b2, char* ws, u16* __restrict__ out) {
  __shared__ __attribute__((aligned(16))) char smem[L_END];
  LAS char* sm = (LAS char*)smem;
  const int b = blockIdx.x, tid = threadIdx.x, l = tid & 63;
  const int w = __builtin_amdgcn_readfirstlane(tid >> 6);
  const int xcd = b & 7, i = b >> 3, pos = xcd * 32 + i;
  const int c = l & 15, g = l >> 4;
  const rsrc_t rw = __builtin_amdgcn_make_buffer_rsrc(ws, 0, OFF_END, 0x00020000);
  const f32x4 z4 = {0.f, 0.f, 0.f, 0.f};
  STAMP_DECL
  STAMP(0);
  const int s0 = 32 * (32 * (xcd >> 2) + 16 * (w >> 1) + 4 * (xcd & 3) + 2 * (w & 1)) + 8 * ((i & 15) >> 2) + 4 * (i >> 4) + (i & 3);
  const u32x4 xa = *(const u32x4*)(x + (size_t)s0 * D_ + 8 * l);
  const u32x4 xb = *(const u32x4*)(x + (size_t)(s0 + 32) * D_ + 8 * l);
  const unsigned ep = ld1(rw, 0, OFF_E);
  const int j0 = 16 * (pos >> 6) + (pos & 15) + 512 * ((pos >> 4) & 3);
  u16 fr[8], fi[8];
#pragma unroll
  for (int e = 0; e < 8; ++e) {
    fr[e] = wr[(size_t)(j0 + 64 * e) * DF_ + 1 + tid];
    fi[e] = wi[(size_t)(j0 + 64 * e) * DF_ + 1 + tid];
  }
  const int kp = 8 * ((pos & 15) + 16 * (pos >> 6)) + 4 * ((pos >> 4) & 1) + 2 * ((pos >> 5) & 1);
  const int jc = 16 * w + c;
  const unsigned vh = (jc >> 1) * 65536 + (jc & 1) * 64 + g * 16;
  st4(u32x4{~0u, ~0u, ~0u, ~0u}, rw, vh, OFF_H + kp * 128);
  st4(u32x4{~0u, ~0u, ~0u, ~0u}, rw, vh + 128, OFF_H + kp * 128);
  const int j1 = xcd * 22 + i;
  const bool has1 = i < 22 && j1 < F_ / 8;
  const int j2 = 3 * pos + w;
  const bool has2 = w < 3 && j2 < KS2 * 8;
  u32x4 v0, v1, y0, y1;
  {
    const u16* p1 = w1 + (size_t)tid * F_ + 8 * (has1 ? j1 : 0);
    v0 = *(const u32x4*)p1;
    v1 = *(const u32x4*)(p1 + 256 * F_);
    const int jj = has2 ? j2 : 0;
    const u16* p2 = w2 + (size_t)(16 * (jj >> 3) + (l >> 3)) * D_ + 64 * (jj & 7) + 8 * (l & 7);
    y0 = *(const u32x4*)p2;
    y1 = *(const u32x4*)(p2 + 8 * D_);
  }
  fence();
  RowDft q;
  rowdft_init(q, l);
  fence();
  const unsigned tag = __builtin_amdgcn_readfirstlane(ep) + 1u;
  {
    LAS char* mine = sm + L_TMP + w * 2048;
    *(LAS u32x4*)(mine + l * 16) = xa;
    *(LAS u32x4*)(mine + 1024 + l * 16) = xb;
    STAMP(1);
    float rs[2], sdv[2], mu[2];
#pragma unroll
    for (int rr = 0; rr < 2; ++rr) {
      const u32x4 v = rr ? xb : xa;
      float f[8];
#pragma unroll
      for (int e = 0; e < 4; ++e) { f[2 * e] = lo16(v[e]); f[2 * e + 1] = hi16(v[e]); }
      mu[rr] = wave_sum(((f[0] + f[1]) + (f[2] + f[3])) + ((f[4] + f[5]) + (f[6] + f[7]))) * (1.f / D_);
      float ss = 0.f;
#pragma unroll
      for (int e = 0; e < 8; ++e) { const float dlt = f[e] - mu[rr]; ss += dlt * dlt; }
      const float var = wave_sum(ss) * (1.f / D_) + EPS;
      rs[rr] = __builtin_amdgcn_rsqf(var);
      sdv[rr] = var * rs[rr];
    }
    float o[2][8];
#pragma unroll
    for (int rr = 0; rr < 2; ++rr) {
      const u32x2 a0 = tr_read(mine + rr * 1024 + l * 8), a1 = tr_read(mine + rr * 1024 + 512 + l * 8);
      const u32x4 a = {a0[0], a0[1], a1[0], a1[1]};
      rowdft(q, a, l, o[rr]);
    }
    const unsigned sp = pk(sdv[0], sdv[1]);
#pragma unroll
    for (int e = 0; e < 4; ++e) {
      const int kk = c + 16 * (e >> 1) + 32 * (e & 1) + 64 * g;
      *(LAS u32x4*)(sm + L_H + kk * 64 + ((w ^ ((c >> 1) & 3)) * 16)) =
          u32x4{pk(o[0][2 * e] * rs[0], o[1][2 * e] * rs[1]), pk(o[0][2 * e + 1] * rs[0], o[1][2 * e + 1] * rs[1]), sp, tag};
    }
    unsigned fw[8];
#pragma unroll
    for (int e = 0; e < 8; ++e) fw[e] = (unsigned)fr[e] | ((unsigned)fi[e] << 16);
    *(LAS u32x4*)(sm + L_H + 16384 + tid * 48) = u32x4{fw[0], fw[1], fw[2], tag};
    *(LAS u32x4*)(sm + L_H + 16384 + tid * 48 + 16) = u32x4{fw[3], fw[4], fw[5], tag};
    *(LAS u32x4*)(sm + L_H + 16384 + tid * 48 + 32) = u32x4{fw[6], fw[7], 0u, tag};
    *(LAS u32x4*)(sm + L_BAS + tid * 16) = v0;
    *(LAS u32x4*)(sm + L_BAS + 4096 + tid * 16) = v1;
    drain();
    bar();
    STAMP(2);
#pragma unroll
    for (int it = 0; it < 4; ++it) {
      const int p = tid + 256 * it;
      st4(*(LAS u32x4*)(sm + L_H + (p >> 2) * 64 + (((p & 3) ^ ((p >> 3) & 3)) * 16)), rw, tid * 16 + it * 4096, OFF_Z + pos * ZSTR);
    }
#pragma unroll
    for (int it = 0; it < 3; ++it) st4(*(LAS u32x4*)(sm + L_H + 16384 + tid * 16 + it * 4096), rw, tid * 16 + it * 4096, OFF_W + pos * WSTR);
    if (l < 2) st1(__builtin_bit_cast(unsigned, l ? mu[1] : mu[0]), rw, (s0 + 32 * l) * 4, OFF_MEAN);
  }
  fence();
  STAMP(3);
  if (has1) {
    unsigned dw[8];
#pragma unroll
    for (int j = 0; j < 8; ++j) dw[j] = *(LAS unsigned*)(sm + L_BAS + (16 * (4 * g + (j & 3) + 16 * (j >> 2)) + c) * 16 + w * 4);
#pragma unroll
    for (int rr = 0; rr < 2; ++rr) {
      u32x4 a;
#pragma unroll
      for (int jj = 0; jj < 4; ++jj) a[jj] = __builtin_amdgcn_perm(dw[2 * jj + 1], dw[2 * jj], rr ? 0x07060302u : 0x05040100u);
      float o[8];
      rowdft(q, a, l, o);
      *(LAS u32x4*)(sm + L_BAS + 8192 + l * 128 + (((2 * w + rr) ^ (l & 7)) * 16)) =
          u32x4{pk(o[0], o[1]), pk(o[2], o[3]), pk(o[4], o[5]), pk(o[6], o[7])};
    }
    bar();
#pragma unroll
    for (int it = 0; it < 2; ++it) {
      const int ll = (tid >> 3) + 32 * it;
      st4(*(LAS u32x4*)(sm + L_BAS + 8192 + ll * 128 + (((tid & 7) ^ (ll & 7)) * 16)), rw, ll * 256 + (tid & 7) * 16,
          OFF_W1F + (j1 >> 1) * 16384 + (j1 & 1) * 128);
    }
  }
  if (has2) {
    LAS char* mine = sm + L_TMP + w * 2048;
    *(LAS u32x4*)(mine + l * 16) = y0;
    *(LAS u32x4*)(mine + 1024 + l * 16) = y1;
    LAS char* rd = mine + (8 * (g >> 1) + (c >> 2)) * 128 + (16 * (c & 1) + 8 * (g & 1) + 4 * ((c >> 1) & 1)) * 2;
#pragma unroll
    for (int dt = 0; dt < 2; ++dt) {
      const u32x2 a0 = tr_read(rd + dt * 64), a1 = tr_read(rd + dt * 64 + 512);
      st4(u32x4{a0[0], a0[1], a1[0], a1[1]}, rw, l * 16, OFF_W2F + ((2 * (j2 & 7) + dt) * KS2 + (j2 >> 3)) * 1024);
    }
  }
  fence();
  STAMP(4);
  const int kw = w >> 1;
  u32x4 c1[2], s1[2], c2[2], s2[2], sel;
  float t1r[8], t1i[8], t2r[8], t2i[8];
#pragma unroll
  for (int ks = 0; ks < 2; ++ks)
#pragma unroll
    for (int jj = 0; jj < 4; ++jj) {
      const int n = 32 * (ks ^ kw) + 16 * (jj >> 1) + 4 * g + ((2 * jj) & 3);
      const float r0 = (float)((n * jc) & 63) * (1.f / 64), r1 = (float)(((n + 1) * jc) & 63) * (1.f / 64);
      c1[ks][jj] = pk(cosr(r0), cosr(r1));
      s1[ks][jj] = pk(sinr(r0), sinr(r1));
    }
  if (has1) {
    drain();
    bar();
    if (tid < 8) st1(tag, rw, tid * 1024 + j1 * 4, OFF_AUX);
  }
#pragma unroll
  for (int t = 0; t < 2; ++t) {
    const int sg = 8 * (c >> 2) + 4 * t + (c & 3);
#pragma unroll
    for (int jj = 0; jj < 4; ++jj) {
      const int n = 8 * g + 2 * jj;
      const float r0 = (float)((n * sg) & 31) * (1.f / 32), r1 = (float)(((n + 1) * sg) & 31) * (1.f / 32);
      c2[t][jj] = pk(cosr(r0), cosr(r1));
      s2[t][jj] = pk(sinr(r0), sinr(r1));
    }
#pragma unroll
    for (int r = 0; r < 4; ++r) {
      const float a = (float)((8 * g + 4 * t + r) * jc) * (1.f / 2048);
      t1r[4 * t + r] = cosr(a);
      t1i[4 * t + r] = -sinr(a);
      const float bq = (float)(sg * (16 * w + 4 * g + r)) * (1.f / 2048);
      t2r[4 * t + r] = cosr(bq) * (1.f / 2048);
      t2i[4 * t + r] = sinr(bq) * (1.f / 2048);
    }
  }
#pragma unroll
  for (int jj = 0; jj < 4; ++jj) {
    const bool on = ((jj >> 1) == (w & 1)) && ((c >> 2) == g) && (((c & 3) >> 1) == (jj & 1));
    sel[jj] = on ? ((c & 1) ? 0x3f800000u : 0x00003f80u) : 0u;
  }
  const int grp = 4 * xcd + (i >> 3), cu = i & 7;
  const int T0 = (cu < 6 ? 11 * cu : 10 * cu + 6) + ((cu >= 6 && w == 3) ? 8 : 3 * w);
  const bool three = w < 2 || (w == 2 && cu < 6);
  fence();
  STAMP(5);
  unsigned auxv;
  {
    u32x4 zp[4], wp[3];
    const unsigned vz = (tid >> 2) * ZSTR + (tid & 3) * 16;
    unsigned vw[3];
#pragma unroll
    for (int it = 0; it < 3; ++it) { const unsigned p = tid + 256 * it, pp = p / 3u; vw[it] = pp * WSTR + (p - 3 * pp) * 16; }
    const unsigned va = (2 * T0 + (l < (three ? 6 : 4) ? l : 0)) * 4;
    for (int spin = 0; spin < SPIN; ++spin) {
      asm volatile("" ::: "memory");
#pragma unroll
      for (int it = 0; it < 4; ++it) zp[it] = ld4(rw, vz, OFF_Z + pos * 64 + it * 64 * ZSTR);
#pragma unroll
      for (int it = 0; it < 3; ++it) wp[it] = ld4(rw, vw[it], OFF_W + pos * 48);
      auxv = ld1(rw, va, OFF_AUX + xcd * 1024);
      const bool bad = zp[0][3] != tag || zp[1][3] != tag || zp[2][3] != tag || zp[3][3] != tag || wp[0][3] != tag || wp[1][3] != tag || wp[2][3] != tag;
      if (!any(bad)) break;
    }
    STAMP(6);
#pragma unroll
    for (int it = 0; it < 4; ++it) {
      const int pp = 64 * it + (tid >> 2), qq = tid & 3, px = pp >> 5, pi = pp & 31;
      *(LAS unsigned*)(sm + L_TMP + 8192 + it * 1024 + tid * 4) = zp[it][0];
      *(LAS unsigned*)(sm + L_TMP + 12288 + it * 1024 + tid * 4) = zp[it][1];
      const int s = 32 * (32 * (px >> 2) + 16 * (qq >> 1) + 4 * (px & 3) + 2 * (qq & 1)) + 8 * ((pi & 15) >> 2) + 4 * (pi >> 4) + (pi & 3);
      *(LAS u16*)(sm + L_TMP + 16384 + s * 2) = (u16)zp[it][2];
      *(LAS u16*)(sm + L_TMP + 16384 + s * 2 + 64) = (u16)(zp[it][2] >> 16);
    }
#pragma unroll
    for (int it = 0; it < 3; ++it) *(LAS u32x4*)(sm + L_TMP + 20480 + it * 4096 + tid * 16) = wp[it];
    drain();
    bar();
    if (b == 0 && tid == 0) st1(tag, rw, 0, OFF_E);
    for (int spin = 0; spin < SPIN; ++spin) {
      if (!any(auxv != tag)) break;
      asm volatile("" ::: "memory");
      auxv = ld1(rw, va, OFF_AUX + xcd * 1024);
    }
  }
  fence();
  u32x4 wa[3][16];
#pragma unroll
  for (int ks = 0; ks < 16; ++ks) {
    wa[0][ks] = ld4(rw, l * 16 + (ks & 3) * 1024, OFF_W1F + T0 * 16384 + (ks >> 2) * 4096);
    wa[1][ks] = ld4(rw, l * 16 + (ks & 3) * 1024, OFF_W1F + T0 * 16384 + 16384 + (ks >> 2) * 4096);
    wa[2][ks] = ld4(rw, (three ? l * 16 : 0x40000000) + (ks & 3) * 1024, OFF_W1F + T0 * 16384 + 32768 + (ks >> 2) * 4096);
  }
  fence();
  STAMP(7);
  {
    u32x4 zr[2][2], zi[2][2];
#pragma unroll
    for (int ks = 0; ks < 2; ++ks)
#pragma unroll
      for (int t = 0; t < 2; ++t) {
        LAS char* src = sm + L_TMP + 8192 + ((((ks ^ kw) * 4 + g) * 32) + 16 * t + c) * 16;
        zr[t][ks] = *(LAS u32x4*)src;
        zi[t][ks] = *(LAS u32x4*)(src + 4096);
      }
    const u32x4 sd = *(LAS u32x4*)(sm + L_TMP + 16384 + (32 * jc + 8 * g) * 2);
    const u32x4 wq0 = *(LAS u32x4*)(sm + L_TMP + 20480 + tid * 48), wq1 = *(LAS u32x4*)(sm + L_TMP + 20480 + tid * 48 + 16),
                wq2 = *(LAS u32x4*)(sm + L_TMP + 20480 + tid * 48 + 32);
    const u32x4 wf0 = {wq0[0], wq0[1], wq0[2], wq1[0]}, wf1 = {wq1[1], wq1[2], wq2[0], wq2[1]};
    u32x4 bre, bim;
    f32x4 hr[2], hi[2];
#pragma unroll
    for (int t = 0; t < 2; ++t) {
      f32x4 tr = mma16(zr[t][0], c1[0], z4);
      f32x4 ti = mma16(zi[t][0], c1[0], z4);
      tr = mma16(zi[t][0], s1[0], tr);
      ti = mma16(zr[t][0], neg4(s1[0]), ti);
      tr = mma16(zr[t][1], c1[1], tr);
      ti = mma16(zi[t][1], c1[1], ti);
      tr = mma16(zi[t][1], s1[1], tr);
      ti = mma16(zr[t][1], neg4(s1[1]), ti);
      float ur[4], ui[4];
#pragma unroll
      for (int r = 0; r < 4; ++r) {
        ur[r] = tr[r] * t1r[4 * t + r] - ti[r] * t1i[4 * t + r];
        ui[r] = tr[r] * t1i[4 * t + r] + ti[r] * t1r[4 * t + r];
      }
      bre[2 * t] = pk(ur[0], ur[1]); bre[2 * t + 1] = pk(ur[2], ur[3]);
      bim[2 * t] = pk(ui[0], ui[1]); bim[2 * t + 1] = pk(ui[2], ui[3]);
      const unsigned sa = sd[2 * t], sb = sd[2 * t + 1];
      const f32x4 sv = {lo16(sa), hi16(sa), lo16(sb), hi16(sb)};
      hr[t] = mma16(zr[t][0], sel, z4) * sv;
      hi[t] = mma16(zi[t][0], sel, z4) * sv;
    }
    u32x4 are, aim;
#pragma unroll
    for (int t = 0; t < 2; ++t) {
      f32x4 xr = mma16(c2[t], bre, z4);
      f32x4 xi = mma16(c2[t], bim, z4);
      xr = mma16(s2[t], bim, xr);
      xi = mma16(neg4(s2[t]), bre, xi);
      const u32x4 wf = t ? wf1 : wf0;
      float ur[4], ui[4];
#pragma unroll
      for (int r = 0; r < 4; ++r) {
        const float a = lo16(wf[r]), bq = hi16(wf[r]);
        ur[r] = xr[r] * a - xi[r] * bq;
        ui[r] = xr[r] * bq + xi[r] * a;
      }
      are[2 * t] = pk(ur[0], ur[1]); are[2 * t + 1] = pk(ur[2], ur[3]);
      aim[2 * t] = pk(ui[0], ui[1]); aim[2 * t + 1] = pk(ui[2], ui[3]);
    }
#pragma unroll
    for (int t = 0; t < 2; ++t) {
      f32x4 vr = mma16(are, c2[t], z4);
      f32x4 vi = mma16(aim, c2[t], z4);
      vr = mma16(aim, neg4(s2[t]), vr);
      vi = mma16(are, s2[t], vi);
      float ur[4], ui[4];
#pragma unroll
      for (int r = 0; r < 4; ++r) {
        ur[r] = vr[r] * t2r[4 * t + r] - vi[r] * t2i[4 * t + r];
        ui[r] = vr[r] * t2i[4 * t + r] + vi[r] * t2r[4 * t + r];
      }
      *(LAS u32x4*)(sm + L_TMP + (w * 2 + t) * 1024 + l * 16) = u32x4{pk(ur[0], ur[1]), pk(ur[2], ur[3]), pk(ui[0], ui[1]), pk(ui[2], ui[3])};
    }
    bar();
#pragma unroll
    for (int ks = 0; ks < 2; ++ks)
#pragma unroll
      for (int hh = 0; hh < 2; ++hh) {
        const int src = 2 * (ks ^ kw) + hh;
        const u32x4 br = {c1[ks][2 * hh], c1[ks][2 * hh + 1], s1[ks][2 * hh] ^ 0x80008000u, s1[ks][2 * hh + 1] ^ 0x80008000u};
        const u32x4 bi = {s1[ks][2 * hh], s1[ks][2 * hh + 1], c1[ks][2 * hh], c1[ks][2 * hh + 1]};
#pragma unroll
        for (int t = 0; t < 2; ++t) {
          const u32x4 u = *(LAS u32x4*)(sm + L_TMP + (src * 2 + t) * 1024 + l * 16);
          hr[t] = mma16(u, br, hr[t]);
          hi[t] = mma16(u, bi, hi[t]);
        }
      }
    const u32x4 ore = {pk(hr[0][0], hr[0][1]), pk(hr[0][2], hr[0][3]), pk(hr[1][0], hr[1][1]), pk(hr[1][2], hr[1][3])};
    u32x4 oim = {pk(hi[0][0], hi[0][1]), pk(hi[0][2], hi[0][3]), pk(hi[1][0], hi[1][1]), pk(hi[1][2], hi[1][3])};
    if (pos == 255) oim = u32x4{0u, 0u, 0u, 0u};
    st4(clean(ore), rw, vh, OFF_H + kp * 128);
    st4(clean(oim), rw, vh + 128, OFF_H + kp * 128);
  }
  fence();
  STAMP(8);
  const int n = l & 31, h = l >> 5;
#pragma unroll
  for (int dd = 0; dd < 2; ++dd) {
    const int d = 64 * cu + 32 * (dd ^ (w & 1)) + d_of_m(n);
    const float dl = (float)d * (1.f / 512);
    const float cd = cosr(dl), sn = sinr(dl);
    const f32x2 rc = {cd, cd}, rs = {sn, -sn};
    f32x2 p[2];
#pragma unroll
    for (int r = 0; r < 2; ++r) {
      const float a = (float)(((1 + 16 * h + 32 * r + 64 * w) * d) & 511) * (1.f / 512);
      p[r] = f32x2{cosr(a) * 0.0625f, sinr(a) * -0.0625f};
    }
#pragma unroll
    for (int ii = 0; ii < 8; ++ii) {
      u32x4 f;
#pragma unroll
      for (int r = 0; r < 2; ++r) {
        f[2 * r] = pk(p[r][0], p[r][1]);
        p[r] = p[r] * rc + f32x2{p[r][1], p[r][0]} * rs;
        if (ii == 7 && r == 1) p[r][0] *= (w == 3 && h == 1) ? 0.70710678118654752f : 1.f;
        f[2 * r + 1] = pk(p[r][0], p[r][1]);
        p[r] = p[r] * rc + f32x2{p[r][1], p[r][0]} * rs;
      }
      *(LAS u32x4*)(sm + L_BAS + w * 16384 + (2 * ii + dd) * 1024 + l * 16) = f;
    }
  }
  fence();
  STAMP(9);
  {
    u32x4 hp[16];
    for (int spin = 0; spin < SPIN; ++spin) {
      asm volatile("" ::: "memory");
#pragma unroll
      for (int it = 0; it < 16; ++it) hp[it] = ld4(rw, tid * 16, OFF_H + grp * 65536 + it * 4096);
      unsigned m = 0;
#pragma unroll
      for (int it = 0; it < 16; ++it) m = umax(umax(m, umax(hp[it][0], hp[it][1])), umax(hp[it][2], hp[it][3]));
      if (!any(m == ~0u)) break;
    }
    STAMP(10);
    const int row = tid >> 3, f = ((row >> 1) & 1) | (((row >> 3) & 1) << 1);
    LAS char* dst = sm + L_H + row * 128 + ((((tid & 7) >> 1) ^ f) * 32) + (tid & 1) * 16;
#pragma unroll
    for (int it = 0; it < 16; ++it) *(LAS u32x4*)(dst + it * 4096) = hp[it];
    bar();
  }
  fence();
  const int k0 = 22 * w;
  u32x4 fv[2][22];
  u32x2 e1[3];
#pragma unroll
  for (int t = 0; t < 3; ++t) e1[t] = *(const u32x2*)(b1 + 16 * (T0 + ((t < 2 || three) ? t : 0)) + 4 * g);
#pragma unroll
  for (int k = 0; k < 22; ++k)
#pragma unroll
    for (int dd = 0; dd < 2; ++dd)
      fv[dd][k] = ld4(rw, ((k < 20 || w < 3) ? l * 16 : 0x40000000) + (k & 3) * 1024, OFF_W2F + ((2 * cu + (dd ^ (w & 1))) * KS2 + k0 + (k & ~3)) * 1024);
  fence();
  {
    f32x4 acc[3][4];
    float ssq[4] = {0.f, 0.f, 0.f, 0.f};
#pragma unroll
    for (int t = 0; t < 3; ++t)
#pragma unroll
      for (int st = 0; st < 4; ++st) acc[t][st] = z4;
    const int f = ((c >> 3) & 1) | ((g & 1) << 1);
    LAS char* rd[4];
#pragma unroll
    for (int st = 0; st < 4; ++st) rd[st] = sm + L_H + (8 * g + (c >> 2)) * 128 + ((st ^ f) * 32) + (c & 3) * 8;
    u32x4 hb[2][4];
#pragma unroll
    for (int st = 0; st < 4; ++st) {
      const u32x2 a0 = tr_read(rd[st]), a1 = tr_read(rd[st] + 512);
      hb[0][st] = u32x4{a0[0], a0[1], a1[0], a1[1]};
    }
#pragma unroll
    for (int ks = 0; ks < 16; ++ks) {
      if (ks < 15) {
#pragma unroll
        for (int st = 0; st < 4; ++st) {
          const u32x2 a0 = tr_read(rd[st] + (ks + 1) * 4096), a1 = tr_read(rd[st] + (ks + 1) * 4096 + 512);
          hb[(ks + 1) & 1][st] = u32x4{a0[0], a0[1], a1[0], a1[1]};
        }
      }
      __builtin_amdgcn_sched_barrier(0);
#pragma unroll
      for (int st = 0; st < 4; ++st) {
#pragma unroll
        for (int t = 0; t < 3; ++t) acc[t][st] = mma16(wa[t][ks], hb[ks & 1][st], acc[t][st]);
#pragma unroll
        for (int e = 0; e < 4; ++e) ssq[st] = sq2(hb[ks & 1][st][e], ssq[st]);
      }
      __builtin_amdgcn_sched_barrier(0);
    }
    STAMP(11);
    float rstd[4];
#pragma unroll
    for (int st = 0; st < 4; ++st) rstd[st] = __builtin_amdgcn_rsqf(xsum(ssq[st]) * (1.f / D_) + EPS);
#pragma unroll
    for (int t = 0; t < 3; ++t) {
      const unsigned ea = e1[t][0], eb = e1[t][1];
      const float bias[4] = {lo16(ea), hi16(ea), lo16(eb), hi16(eb)};
      unsigned gp[4][2];
#pragma unroll
      for (int st = 0; st < 4; ++st) {
        float gv[4];
#pragma unroll
        for (int r = 0; r < 4; ++r) {
          const float pre = acc[t][st][r] * rstd[st] + bias[r];
          const float p = pre > 0.f ? pre : 0.f;
          gv[r] = p * p;
        }
        gp[st][0] = pk(gv[0], gv[1]); gp[st][1] = pk(gv[2], gv[3]);
      }
#pragma unroll
      for (int sp = 0; sp < 2; ++sp) {
        const auto a = __builtin_amdgcn_permlane16_swap(gp[2 * sp][0], gp[2 * sp + 1][0], false, false);
        const auto bq = __builtin_amdgcn_permlane16_swap(gp[2 * sp][1], gp[2 * sp + 1][1], false, false);
        st4(u32x4{a[0], bq[0], a[1], bq[1]}, rw, ((t < 2 || three) ? l * 16 : 0x40000000) + sp * 1024, OFF_G + (grp * KS2 + T0 + t) * 2048);
      }
    }
  }
  fence();
  f32x16 acc[4];
#pragma unroll
  for (int t = 0; t < 4; ++t)
#pragma unroll
    for (int r = 0; r < 16; ++r) acc[t][r] = 0.f;
  {
    const int qq = c >> 2;
    LAS char* rb = sm + L_H + (128 * w + (qq & 1) + 8 * (qq >> 1) + 4 * h) * 128 + (c & 3) * 8;
    LAS char* rd[2][2];
#pragma unroll
    for (int ss = 0; ss < 2; ++ss)
#pragma unroll
      for (int r = 0; r < 2; ++r) rd[ss][r] = rb + r * 256 + (((2 * (ss ^ (w >> 1)) + (g & 1)) ^ (r | (qq & 2))) * 32);
#pragma unroll
    for (int ii = 0; ii < 8; ++ii) {
      u32x4 hf[2], bf[2];
#pragma unroll
      for (int ss = 0; ss < 2; ++ss) {
        const u32x2 a0 = tr_read(rd[ss][0] + ii * 2048), a1 = tr_read(rd[ss][1] + ii * 2048);
        hf[ss] = u32x4{a0[0], a0[1], a1[0], a1[1]};
        bf[ss] = *(LAS u32x4*)(sm + L_BAS + w * 16384 + (2 * ii + ss) * 1024 + l * 16);
      }
      acc[0] = mma32(bf[0], hf[0], acc[0]);
      acc[2] = mma32(bf[0], hf[1], acc[2]);
      acc[1] = mma32(bf[1], hf[0], acc[1]);
      acc[3] = mma32(bf[1], hf[1], acc[3]);
    }
  }
  fence();
  drain();
  if (l < 8) st1(tag, rw, l * 128 + (4 * cu + w) * 4, OFF_F3 + grp * 1024);
  STAMP(12);
  const int srow = 64 * grp + 32 * (w >> 1) + n, dcol = 64 * cu + 32 * (w & 1) + 16 * h;
  const unsigned mu = ld1(rw, srow * 4, OFF_MEAN);
  const u32x4 e20 = *(const u32x4*)(b2 + dcol), e21 = *(const u32x4*)(b2 + dcol + 8);
  for (int spin = 0; spin < SPIN; ++spin) {
    asm volatile("" ::: "memory");
    const unsigned fl = ld1(rw, (8 * w + (l & 7)) * 4, OFF_F3 + (grp * 8 + cu) * 128);
    if (!any(fl != tag)) break;
  }
  fence();
  STAMP(13);
  {
    u32x4 fg[2][22];
#pragma unroll
    for (int k = 0; k < 22; ++k)
#pragma unroll
      for (int ss = 0; ss < 2; ++ss)
        fg[ss][k] = ld4(rw, ((k < 20 || w < 3) ? l * 16 : 0x40000000) + (k & 1) * 2048, OFF_G + (grp * KS2 + k0 + (k & ~1)) * 2048 + (ss ^ (w >> 1)) * 1024);
    fence();
#pragma unroll
    for (int k = 0; k < 22; ++k) {
      acc[0] = mma32(fv[0][k], fg[0][k], acc[0]);
      acc[1] = mma32(fv[1][k], fg[0][k], acc[1]);
      acc[2] = mma32(fv[0][k], fg[1][k], acc[2]);
      acc[3] = mma32(fv[1][k], fg[1][k], acc[3]);
      __builtin_amdgcn_sched_barrier(0);
    }
  }
  STAMP(14);
#pragma unroll
  for (int j = 1; j < 4; ++j)
#pragma unroll
    for (int qd = 0; qd < 4; ++qd)
      *(LAS f32x4*)(sm + L_BAS + w * 16384 + ((j - 1) * 4 + qd) * 1024 + l * 16) =
          f32x4{acc[j][4 * qd], acc[j][4 * qd + 1], acc[j][4 * qd + 2], acc[j][4 * qd + 3]};
  bar();
  float o[16];
  const float muf = __builtin_bit_cast(float, mu);
#pragma unroll
  for (int qd = 0; qd < 4; ++qd) {
    const f32x4 p1 = *(LAS f32x4*)(sm + L_BAS + (w ^ 1) * 16384 + (0 + qd) * 1024 + l * 16);
    const f32x4 p2 = *(LAS f32x4*)(sm + L_BAS + (w ^ 2) * 16384 + (4 + qd) * 1024 + l * 16);
    const f32x4 p3 = *(LAS f32x4*)(sm + L_BAS + (w ^ 3) * 16384 + (8 + qd) * 1024 + l * 16);
#pragma unroll
    for (int r = 0; r < 4; ++r) o[4 * qd + r] = (acc[0][4 * qd + r] + p1[r]) + (p2[r] + p3[r]) + muf;
  }
#pragma unroll
  for (int e = 0; e < 4; ++e) {
    o[2 * e] += lo16(e20[e]); o[2 * e + 1] += hi16(e20[e]);
    o[8 + 2 * e] += lo16(e21[e]); o[8 + 2 * e + 1] += hi16(e21[e]);
  }
  u16* dst = out + (size_t)srow * D_ + dcol;
  *(u32x4*)dst = u32x4{pk(o[0], o[1]), pk(o[2], o[3]), pk(o[4], o[5]), pk(o[6], o[7])};
  *(u32x4*)(dst + 8) = u32x4{pk(o[8], o[9]), pk(o[10], o[11]), pk(o[12], o[13]), pk(o[14], o[15])};
  STAMP(15);
  STAMP_FLUSH
}

extern "C" void kernel_launch(void* const* d_in, const int* in_sizes, int n_in, void* d_out, int out_size, void* d_ws,
                              size_t ws_size, hipStream_t stream) {
  fused<<<256, 256, 0, stream>>>((const u16*)d_in[0], (const u16*)d_in[1], (const u16*)d_in[2], (const u16*)d_in[4], (const u16*)d_in[5],
                                 (const u16*)d_in[6], (const u16*)d_in[7], (char*)d_ws, (u16*)d_out);
}
```

```cpp
#include <hip/hip_runtime.h>
#include <hip/hip_bf16.h>
#include <stdint.h>

typedef __attribute__((ext_vector_type(8))) __bf16 bf16x8;
typedef __attribute__((ext_vector_type(2))) __bf16 bf16x2;
typedef __attribute__((ext_vector_type(4))) short s16x4;
typedef __attribute__((ext_vector_type(4))) float f32x4;
typedef __attribute__((ext_vector_type(16))) float f32x16;
typedef __attribute__((ext_vector_type(4))) unsigned u32x4;
typedef __attribute__((ext_vector_type(2))) unsigned u32x2;
typedef unsigned short u16;
#define LAS __attribute__((address_space(3)))
#define DEV __device__ __forceinline__

typedef __attribute__((ext_vector_type(2))) float f32x2;
#define STAMP_DECL
#define STAMP(i)
#define STAMP_FLUSH

constexpr int S_ = 2048, D_ = 512, F_ = 1376, DF_ = 257;
constexpr int KS2 = F_ / 16;
constexpr float EPS = 1e-5f;
constexpr int SPIN = 1 << 16;
#define GD 8
#define NW2 4
#define NA 0
#define NB2 6

constexpr unsigned ZSTR = 16384 + 256, WSTR = 12288 + 256;
constexpr unsigned OFF_E = 0;
constexpr unsigned OFF_AUX = 1024;
constexpr unsigned OFF_F3 = 16384;
constexpr unsigned OFF_MEAN = OFF_F3 + 32768;
constexpr unsigned OFF_Z = 65536;
constexpr unsigned OFF_W = OFF_Z + 256 * ZSTR;
constexpr unsigned OFF_H = OFF_W + 256 * WSTR;
constexpr unsigned OFF_W1F = OFF_H + 2097152;
constexpr unsigned OFF_W2F = OFF_W1F + KS2 * 16384;
constexpr unsigned OFF_G = OFF_W2F + 16 * KS2 * 1024;
constexpr unsigned OFF_END = OFF_G + 32 * KS2 * 2048;

constexpr int L_H = 0, L_BAS = 65536, L_TMP = 131072, L_END = 163840;

DEV unsigned pk(float a, float b) {
  bf16x2 v; v.x = (__bf16)a; v.y = (__bf16)b;
  return __builtin_bit_cast(unsigned, v);
}
DEV float lo16(unsigned u) { return __builtin_bit_cast(float, u << 16); }
DEV float hi16(unsigned u) { return __builtin_bit_cast(float, u & 0xffff0000u); }
DEV float cosr(float rev) { return __builtin_amdgcn_cosf(rev); }
DEV float sinr(float rev) { return __builtin_amdgcn_sinf(rev); }
DEV f32x4 mma16(u32x4 a, u32x4 b, f32x4 c) {
  return __builtin_amdgcn_mfma_f32_16x16x32_bf16(__builtin_bit_cast(bf16x8, a), __builtin_bit_cast(bf16x8, b), c, 0, 0, 0);
}
DEV f32x16 mma32(u32x4 a, u32x4 b, f32x16 c) {
  return __builtin_amdgcn_mfma_f32_32x32x16_bf16(__builtin_bit_cast(bf16x8, a), __builtin_bit_cast(bf16x8, b), c, 0, 0, 0);
}
DEV u32x2 tr_read(LAS char* p) {
  return __builtin_bit_cast(u32x2, __builtin_amdgcn_ds_read_tr16_b64_v4i16((LAS s16x4*)p));
}
DEV float sq2(unsigned u, float acc) {
  const bf16x2 v = __builtin_bit_cast(bf16x2, u);
  return __builtin_amdgcn_fdot2_f32_bf16(v, v, acc, false);
}
DEV u32x4 neg4(u32x4 v) { return v ^ 0x80008000u; }
template <int CTRL> DEV float dpp_add(float v) {
  return v + __builtin_bit_cast(float, __builtin_amdgcn_update_dpp(0, __builtin_bit_cast(int, v), CTRL, 0xf, 0xf, true));
}
DEV float wave_sum(float v) {
  v = dpp_add<0xB1>(v);
  v = dpp_add<0x4E>(v);
  v = dpp_add<0x141>(v);
  v = dpp_add<0x140>(v);
  const int i = __builtin_bit_cast(int, v);
  return __builtin_bit_cast(float, __builtin_amdgcn_readlane(i, 0)) + __builtin_bit_cast(float, __builtin_amdgcn_readlane(i, 16)) +
         (__builtin_bit_cast(float, __builtin_amdgcn_readlane(i, 32)) + __builtin_bit_cast(float, __builtin_amdgcn_readlane(i, 48)));
}

struct RowDft {
  u32x4 c1[2], s1[2], a2;
  float twr[8], twi[8];
};
DEV void rowdft_init(RowDft& q, int l) {
  const int c = l & 15, g = l >> 4;
  const float fj = (float)(1 + c), fg = (float)(4 * g);
  const unsigned odd = (c & 1) ? 0u : 0x80008000u;
#pragma unroll
  for (int jj = 0; jj < 2; ++jj) {
    const float r0 = (fg + (float)(2 * jj)) * fj * (1.f / 32), r1 = r0 + fj * (1.f / 32);
    const unsigned cc = pk(cosr(r0), cosr(r1)), ss = pk(sinr(-r0), sinr(-r1));
    q.c1[0][jj] = cc; q.c1[0][jj + 2] = cc ^ odd;
    q.s1[0][jj] = ss; q.s1[0][jj + 2] = ss ^ odd;
  }
  q.c1[1] = q.c1[0] ^ 0x80000000u;
  q.s1[1] = q.s1[0] ^ 0x80000000u;
#pragma unroll
  for (int t = 0; t < 2; ++t) {
    const float dj = (fj + (float)(16 * t)) * (1.f / 512);
#pragma unroll
    for (int r = 0; r < 4; ++r) {
      const float rv = (fg + (float)r) * dj;
      q.twr[4 * t + r] = cosr(rv);
      q.twi[4 * t + r] = sinr(-rv);
    }
  }
  const float f2 = (float)(c >> 1) * (1.f / 16), sh = (c & 1) ? 0.25f : 0.f;
#pragma unroll
  for (int jj = 0; jj < 2; ++jj) {
    const float r0 = (fg + (float)(2 * jj)) * f2 + sh, r1 = r0 + f2;
    q.a2[jj] = pk(cosr(r0) * (1.f / 16), cosr(r1) * (1.f / 16));
    q.a2[jj + 2] = pk(sinr(r0) * (1.f / 16), sinr(r1) * (1.f / 16));
  }
}
DEV void rowdft(const RowDft& q, u32x4 a, int l, float (&o)[8]) {
  const f32x4 z = {0.f, 0.f, 0.f, 0.f};
#pragma unroll
  for (int t = 0; t < 2; ++t) {
    const f32x4 tr = mma16(a, q.c1[t], z), ti = mma16(a, q.s1[t], z);
    float ur[4], ui[4];
#pragma unroll
    for (int r = 0; r < 4; ++r) {
      ur[r] = tr[r] * q.twr[4 * t + r] - ti[r] * q.twi[4 * t + r];
      ui[r] = tr[r] * q.twi[4 * t + r] + ti[r] * q.twr[4 * t + r];
    }
    const u32x4 b = {pk(ur[0], ur[1]), pk(ur[2], ur[3]), pk(ui[0], ui[1]), pk(ui[2], ui[3])};
    const f32x4 x = mma16(q.a2, b, z);
#pragma unroll
    for (int r = 0; r < 4; ++r) o[4 * t + r] = x[r];
  }
  o[6] *= (l == 63) ? 0.70710678118654752f : 1.f;
  o[7] = (l == 63) ? 0.f : o[7];
}

DEV int d_of_m(int m) { return 16 * ((m >> 2) & 1) + (m & 3) + 4 * (m >> 3); }

typedef __amdgpu_buffer_rsrc_t rsrc_t;
DEV u32x4 ld4(rsrc_t r, unsigned v, unsigned s) { return __builtin_amdgcn_raw_buffer_load_b128(r, v, s, 16); }
DEV unsigned ld1(rsrc_t r, unsigned v, unsigned s) { return __builtin_amdgcn_raw_buffer_load_b32(r, v, s, 16); }
DEV void st4(u32x4 d, rsrc_t r, unsigned v, unsigned s) { __builtin_amdgcn_raw_buffer_store_b128(d, r, v, s, 16); }
DEV void st1(unsigned d, rsrc_t r, unsigned v, unsigned s) { __builtin_amdgcn_raw_buffer_store_b32(d, r, v, s, 16); }
DEV rsrc_t mk(const void* p, unsigned n) { return __builtin_amdgcn_make_buffer_rsrc((void*)p, 0, n, 0x00020000); }
DEV u32x4 in4(rsrc_t r, unsigned v, unsigned s) { return __builtin_amdgcn_raw_buffer_load_b128(r, v, s, 0); }
constexpr unsigned OOB = 0x40000000u;
DEV void drain() { asm volatile("s_waitcnt vmcnt(0)" ::: "memory"); }
DEV void bar() { asm volatile("s_waitcnt lgkmcnt(0)\n\ts_barrier" ::: "memory"); }
DEV void fence() { asm volatile("" ::: "memory"); __builtin_amdgcn_sched_barrier(0); }
template <int N> DEV void waitvm() { asm volatile("s_waitcnt vmcnt(%0)" :: "i"(N) : "memory"); }
DEV void keep(u32x4& v) { asm volatile("" : "+v"(v)); }
DEV void keep(float& v) { asm volatile("" : "+v"(v)); }
DEV bool any(bool p) { return __builtin_amdgcn_ballot_w64(p) != 0; }
DEV unsigned umin(unsigned a, unsigned b) { return a < b ? a : b; }
DEV unsigned umax(unsigned a, unsigned b) { return a > b ? a : b; }
DEV u32x4 clean(u32x4 v) {
  return u32x4{umin(v[0], 0xfffffffeu), umin(v[1], 0xfffffffeu), umin(v[2], 0xfffffffeu), umin(v[3], 0xfffffffeu)};
}
DEV void sum2(float a, float b, float& sa, float& sb) {
  const auto q = __builtin_amdgcn_permlane32_swap(__builtin_bit_cast(unsigned, a), __builtin_bit_cast(unsigned, b), false, false);
  const unsigned q0 = q[0], q1 = q[1];
  float v = __builtin_bit_cast(float, q0) + __builtin_bit_cast(float, q1);
  v = dpp_add<0xB1>(v);
  v = dpp_add<0x4E>(v);
  v = dpp_add<0x141>(v);
  v = dpp_add<0x140>(v);
  const int u = __builtin_bit_cast(int, v);
  sa = __builtin_bit_cast(float, __builtin_amdgcn_readlane(u, 0)) + __builtin_bit_cast(float, __builtin_amdgcn_readlane(u, 16));
  sb = __builtin_bit_cast(float, __builtin_amdgcn_readlane(u, 32)) + __builtin_bit_cast(float, __builtin_amdgcn_readlane(u, 48));
}
DEV float xsum(float v) {
  const unsigned u = __builtin_bit_cast(unsigned, v);
  const auto a = __builtin_amdgcn_permlane16_swap(u, u, false, false);
  const unsigned a0 = a[0], a1 = a[1];
  const float s = __builtin_bit_cast(float, a0) + __builtin_bit_cast(float, a1);
  const unsigned su = __builtin_bit_cast(unsigned, s);
  const auto q = __builtin_amdgcn_permlane32_swap(su, su, false, false);
  const unsigned q0 = q[0], q1 = q[1];
  return __builtin_bit_cast(float, q0) + __builtin_bit_cast(float, q1);
}

__global__ __launch_bounds__(256, 1) void fused(const u16* __restrict__ x, const u16* __restrict__ wr, const u16* __restrict__ wi,
                                                const u16* __restrict__ w1, const u16* __restrict__ b1, const u16* __restrict__ w2,
                                                const u16* __restrict__ b2, char* ws, u16* __restrict__ out) {
  __shared__ __attribute__((aligned(16))) char smem[L_END];
  LAS char* sm = (LAS char*)smem;
  const int b = blockIdx.x, tid = threadIdx.x, l = tid & 63;
  const int w = __builtin_amdgcn_readfirstlane(tid >> 6);
  const int xcd = b & 7, i = b >> 3, pos = xcd * 32 + i;
  const int c = l & 15, g = l >> 4;
  const rsrc_t rw = __builtin_amdgcn_make_buffer_rsrc(ws, 0, OFF_END, 0x00020000);
  const f32x4 z4 = {0.f, 0.f, 0.f, 0.f};
  asm volatile("" :: "s"(x), "s"(wr), "s"(wi), "s"(w1), "s"(b1), "s"(w2), "s"(b2), "s"(ws), "s"(out));
  STAMP_DECL
  STAMP(0);
  const int s0 = 32 * (32 * (xcd >> 2) + 16 * (w >> 1) + 4 * (xcd & 3) + 2 * (w & 1)) + 8 * ((i & 15) >> 2) + 4 * (i >> 4) + (i & 3);
  const rsrc_t rx = mk(x, S_ * D_ * 2);
  const u32x4 xa = in4(rx, l * 16, s0 * (D_ * 2)), xb = in4(rx, l * 16, (s0 + 32) * (D_ * 2));
  const unsigned ep = ld1(rw, 0, OFF_E);
  fence();
  const int kp = 8 * ((pos & 15) + 16 * (pos >> 6)) + 4 * ((pos >> 4) & 1) + 2 * ((pos >> 5) & 1);
  const int jc = 16 * w + c;
  const unsigned vh = (jc >> 1) * 65536 + (jc & 1) * 64 + g * 16;
  st4(u32x4{~0u, ~0u, ~0u, ~0u}, rw, vh, OFF_H + kp * 128);
  st4(u32x4{~0u, ~0u, ~0u, ~0u}, rw, vh + 128, OFF_H + kp * 128);
  fence();
  const int j0 = 16 * (pos >> 6) + (pos & 15) + 512 * ((pos >> 4) & 3);
  u16 fr[8], fi[8];
  {
    const rsrc_t r0 = __builtin_amdgcn_make_buffer_rsrc((void*)wr, 0, S_ * DF_ * 2, 0x00020000);
    const rsrc_t r1 = __builtin_amdgcn_make_buffer_rsrc((void*)wi, 0, S_ * DF_ * 2, 0x00020000);
#pragma unroll
    for (int e = 0; e < 8; ++e) {
      fr[e] = __builtin_amdgcn_raw_buffer_load_b16(r0, 2 + 2 * tid, (j0 + 64 * e) * (DF_ * 2), 0);
      fi[e] = __builtin_amdgcn_raw_buffer_load_b16(r1, 2 + 2 * tid, (j0 + 64 * e) * (DF_ * 2), 0);
    }
  }
  fence();
  const int j1 = xcd * 22 + i;
  const bool has1 = i < 22 && j1 < F_ / 8;
  const int j2 = 3 * pos + w;
  const bool has2 = w < 3 && j2 < KS2 * 8;
  u32x4 v0, v1, y0, y1;
  unsigned tc0, tc1;
  const rsrc_t r1 = mk(w1, D_ * F_ * 2);
  {
    const rsrc_t r2 = mk(w2, D_ * F_ * 2);
    tc0 = __builtin_amdgcn_raw_buffer_load_b32(r1, l < 16 ? (16 * i + 4 * w + (l >> 2)) * (F_ * 2) + xcd * 352 + umin((l & 3) * 128, 348) : OOB, 0, 0);
    const unsigned o2 = has2 ? (16 * (j2 >> 3) + (l >> 3)) * (D_ * 2) + (j2 & 7) * 128 + (l & 7) * 16 : OOB;
    y0 = in4(r2, o2, 0);
    y1 = in4(r2, o2, 8 * D_ * 2);
  }
  fence();
  RowDft q;
  rowdft_init(q, l);
  keep(q.c1[0]); keep(q.c1[1]); keep(q.s1[0]); keep(q.s1[1]); keep(q.a2);
#pragma unroll
  for (int e = 0; e < 8; ++e) { keep(q.twr[e]); keep(q.twi[e]); }
  fence();
  fence();
  const unsigned tag = __builtin_amdgcn_readfirstlane(ep) + 1u;
  {
    LAS char* mine = sm + L_TMP + w * 2048;
    *(LAS u32x4*)(mine + l * 16) = xa;
    *(LAS u32x4*)(mine + 1024 + l * 16) = xb;
    STAMP(1);
    float o[2][8];
#pragma unroll
    for (int rr = 0; rr < 2; ++rr) {
      const u32x2 a0 = tr_read(mine + rr * 1024 + l * 8), a1 = tr_read(mine + rr * 1024 + 512 + l * 8);
      const u32x4 a = {a0[0], a0[1], a1[0], a1[1]};
      rowdft(q, a, l, o[rr]);
    }
    float rs[2], sdv[2];
    {
      float e0 = 0.f, e1 = 0.f;
#pragma unroll
      for (int e = 0; e < 8; ++e) { e0 += o[0][e] * o[0][e]; e1 += o[1][e] * o[1][e]; }
      float t0, t1;
      sum2(e0, e1, t0, t1);
      const float va = t0 * (1.f / D_) + EPS, vb = t1 * (1.f / D_) + EPS;
      rs[0] = __builtin_amdgcn_rsqf(va); rs[1] = __builtin_amdgcn_rsqf(vb);
      sdv[0] = va * rs[0]; sdv[1] = vb * rs[1];
    }
    const unsigned sp = pk(sdv[0], sdv[1]);
#pragma unroll
    for (int e = 0; e < 4; ++e) {
      const int kk = c + 16 * (e >> 1) + 32 * (e & 1) + 64 * g;
      *(LAS u32x4*)(sm + L_H + kk * 64 + ((w ^ ((c >> 1) & 3)) * 16)) =
          u32x4{pk(o[0][2 * e] * rs[0], o[1][2 * e] * rs[1]), pk(o[0][2 * e + 1] * rs[0], o[1][2 * e + 1] * rs[1]), sp, tag};
    }
    waitvm<19>();
    bar();
    STAMP(2);
#pragma unroll
    for (int it = 0; it < 4; ++it) {
      const int p = tid + 256 * it;
      st4(*(LAS u32x4*)(sm + L_H + (p >> 2) * 64 + (((p & 3) ^ ((p >> 3) & 3)) * 16)), rw, tid * 16 + it * 4096, OFF_Z + pos * ZSTR);
    }
    fence();
    STAMP(3);
    unsigned fw[8];
#pragma unroll
    for (int e = 0; e < 8; ++e) fw[e] = (unsigned)fr[e] | ((unsigned)fi[e] << 16);
    *(LAS u32x4*)(sm + L_H + 16384 + tid * 48) = u32x4{fw[0], fw[1], fw[2], tag};
    *(LAS u32x4*)(sm + L_H + 16384 + tid * 48 + 16) = u32x4{fw[3], fw[4], fw[5], tag};
    *(LAS u32x4*)(sm + L_H + 16384 + tid * 48 + 32) = u32x4{fw[6], fw[7], 0u, tag};
#pragma unroll
    for (int it = 0; it < 3; ++it)
      st4(*(LAS u32x4*)(sm + L_H + 16384 + w * 3072 + l * 16 + it * 1024), rw, w * 3072 + l * 16 + it * 1024, OFF_W + pos * WSTR);
  }
  fence();
  asm volatile("" :: "v"(tc0));
  v0 = in4(r1, has1 ? tid * (F_ * 2) + j1 * 16 : OOB, 0);
  v1 = in4(r1, has1 ? tid * (F_ * 2) + j1 * 16 : OOB, 256 * F_ * 2);
  fence();
  const int kw = w >> 1;
  u32x4 c1[2], s1[2], c2[2], s2[2], sel;
  float t1r[8], t1i[8], t2r[8], t2i[8];
  const float fjc = (float)jc, fg = (float)g;
  {
    const float dj = fjc * (1.f / 64), n0 = 4.f * fg + (float)(32 * kw);
#pragma unroll
    for (int jj = 0; jj < 4; ++jj) {
      const float r0 = (n0 + (float)(16 * (jj >> 1) + ((2 * jj) & 3))) * dj, r1 = r0 + dj;
      c1[0][jj] = pk(cosr(r0), cosr(r1));
      s1[0][jj] = pk(sinr(r0), sinr(r1));
    }
    const unsigned odd = (c & 1) ? 0x80008000u : 0u;
    c1[1] = c1[0] ^ odd;
    s1[1] = s1[0] ^ odd;
  }
  keep(c1[0]); keep(c1[1]); keep(s1[0]); keep(s1[1]);
#pragma unroll
  for (int t = 0; t < 2; ++t) {
    const float sg = (float)(8 * (c >> 2) + 4 * t + (c & 3));
#pragma unroll
    for (int jj = 0; jj < 4; ++jj) {
      const float r0 = (8.f * fg + (float)(2 * jj)) * (sg * (1.f / 32)), r1 = r0 + sg * (1.f / 32);
      c2[t][jj] = pk(cosr(r0), cosr(r1));
      s2[t][jj] = pk(sinr(r0), sinr(r1));
    }
#pragma unroll
    for (int r = 0; r < 4; ++r) {
      const float a = (8.f * fg + (float)(4 * t + r)) * (fjc * (1.f / 2048));
      t1r[4 * t + r] = cosr(a);
      t1i[4 * t + r] = sinr(-a);
      const float bq = (4.f * fg + (float)(16 * w + r)) * (sg * (1.f / 2048));
      t2r[4 * t + r] = cosr(bq) * (1.f / 2048);
      t2i[4 * t + r] = sinr(bq) * (1.f / 2048);
    }
  }
#pragma unroll
  for (int jj = 0; jj < 4; ++jj) {
    const bool on = ((jj >> 1) == (w & 1)) && ((c >> 2) == g) && (((c & 3) >> 1) == (jj & 1));
    sel[jj] = on ? ((c & 1) ? 0x3f800000u : 0x00003f80u) : 0u;
  }
  keep(c2[0]); keep(c2[1]); keep(s2[0]); keep(s2[1]); keep(sel);
#pragma unroll
  for (int e = 0; e < 8; ++e) { keep(t1r[e]); keep(t1i[e]); keep(t2r[e]); keep(t2i[e]); }
  fence();
  STAMP(4);
  const int grp = 4 * xcd + (i >> 3), cu = i & 7;
  const int T0 = (cu < 6 ? 11 * cu : 10 * cu + 6) + ((cu >= 6 && w == 3) ? 8 : 3 * w);
  const bool three = w < 2 || (w == 2 && cu < 6);
  fence();
  {
    u32x4 zp[4], wp[3];
    const unsigned vz = (tid >> 2) * ZSTR + (tid & 3) * 16;
    unsigned vw[3];
#pragma unroll
    for (int it = 0; it < 3; ++it) { const unsigned p = tid + 256 * it, pp = p / 3u; vw[it] = pp * WSTR + (p - 3 * pp) * 16; }
#define POLL1                                                                                            \
    _Pragma("unroll") for (int it = 0; it < 4; ++it) zp[it] = ld4(rw, vz, OFF_Z + pos * 64 + it * 64 * ZSTR); \
    _Pragma("unroll") for (int it = 0; it < 3; ++it) wp[it] = ld4(rw, vw[it], OFF_W + pos * 48);
    POLL1
    fence();
  if (has1) {
    *(LAS u32x4*)(sm + L_BAS + tid * 16) = v0;
    *(LAS u32x4*)(sm + L_BAS + 4096 + tid * 16) = v1;
    bar();
    unsigned dw[8];
#pragma unroll
    for (int j = 0; j < 8; ++j) dw[j] = *(LAS unsigned*)(sm + L_BAS + (16 * (4 * g + (j & 3) + 16 * (j >> 2)) + c) * 16 + w * 4);
#pragma unroll
    for (int rr = 0; rr < 2; ++rr) {
      u32x4 a;
#pragma unroll
      for (int jj = 0; jj < 4; ++jj) a[jj] = __builtin_amdgcn_perm(dw[2 * jj + 1], dw[2 * jj], rr ? 0x07060302u : 0x05040100u);
      float o[8];
      rowdft(q, a, l, o);
      *(LAS u32x4*)(sm + L_BAS + 8192 + l * 128 + (((2 * w + rr) ^ (l & 7)) * 16)) =
          u32x4{pk(o[0], o[1]), pk(o[2], o[3]), pk(o[4], o[5]), pk(o[6], o[7])};
    }
    bar();
  }
#pragma unroll
  for (int it = 0; it < 2; ++it) {
    const int ll = (tid >> 3) + 32 * it;
    st4(*(LAS u32x4*)(sm + L_BAS + 8192 + ll * 128 + (((tid & 7) ^ (ll & 7)) * 16)), rw,
        has1 ? ll * 256 + (tid & 7) * 16 + (j1 >> 1) * 16384 + (j1 & 1) * 128 : OOB, OFF_W1F);
  }
  {
    LAS char* mine = sm + L_TMP + w * 2048;
    *(LAS u32x4*)(mine + l * 16) = y0;
    *(LAS u32x4*)(mine + 1024 + l * 16) = y1;
    LAS char* rd = mine + (8 * (g >> 1) + (c >> 2)) * 128 + (16 * (c & 1) + 8 * (g & 1) + 4 * ((c >> 1) & 1)) * 2;
#pragma unroll
    for (int dt = 0; dt < 2; ++dt) {
      const u32x2 a0 = tr_read(rd + dt * 64), a1 = tr_read(rd + dt * 64 + 512);
      st4(u32x4{a0[0], a0[1], a1[0], a1[1]}, rw, has2 ? l * 16 + ((2 * (j2 & 7) + dt) * KS2 + (j2 >> 3)) * 1024 : OOB, OFF_W2F);
    }
  }
    float m0 = 0.f, m1 = 0.f;
#pragma unroll
    for (int e = 0; e < 4; ++e) { m0 += lo16(xa[e]) + hi16(xa[e]); m1 += lo16(xb[e]) + hi16(xb[e]); }
    sum2(m0, m1, m0, m1);
    st1(__builtin_bit_cast(unsigned, (l ? m1 : m0) * (1.f / D_)), rw, l < 2 ? (s0 + 32 * l) * 4 : OOB, OFF_MEAN);
    fence();
    STAMP(5);
#define BAD1 any((((zp[0][3] ^ tag) | (zp[1][3] ^ tag)) | ((zp[2][3] ^ tag) | (zp[3][3] ^ tag)) | ((wp[0][3] ^ tag) | (wp[1][3] ^ tag)) | (wp[2][3] ^ tag)) != 0)
    if (BAD1) {
#pragma unroll 1
      for (int spin = 0; spin < SPIN; ++spin) {
        asm volatile("" ::: "memory");
        POLL1
        if (!BAD1) break;
      }
    }
    STAMP(6);
#pragma unroll
    for (int it = 0; it < 4; ++it) {
      const int pp = 64 * it + (tid >> 2), qq = tid & 3, px = pp >> 5, pi = pp & 31;
      *(LAS unsigned*)(sm + L_TMP + 8192 + it * 1024 + tid * 4) = zp[it][0];
      *(LAS unsigned*)(sm + L_TMP + 12288 + it * 1024 + tid * 4) = zp[it][1];
      const int s = 32 * (32 * (px >> 2) + 16 * (qq >> 1) + 4 * (px & 3) + 2 * (qq & 1)) + 8 * ((pi & 15) >> 2) + 4 * (pi >> 4) + (pi & 3);
      *(LAS u16*)(sm + L_TMP + 16384 + s * 2) = (u16)zp[it][2];
      *(LAS u16*)(sm + L_TMP + 16384 + s * 2 + 64) = (u16)(zp[it][2] >> 16);
    }
#pragma unroll
    for (int it = 0; it < 3; ++it) *(LAS u32x4*)(sm + L_TMP + 20480 + it * 4096 + tid * 16) = wp[it];
    bar();
    STAMP(18);
  }
  fence();
  u32x4 wa[3][16];
  const int rot = cu & 3;
  const unsigned vw3 = three ? l * 16 : OOB;
#define LDW1(ks)                                                                                         \
  { const unsigned so_ = OFF_W1F + T0 * 16384 + ((((ks) >> 2) + rot) & 3) * 4096;                        \
    wa[0][ks] = ld4(rw, l * 16 + ((ks) & 3) * 1024, so_);                                                \
    wa[1][ks] = ld4(rw, l * 16 + ((ks) & 3) * 1024, so_ + 16384);                                        \
    wa[2][ks] = ld4(rw, vw3 + ((ks) & 3) * 1024, so_ + 32768); }
#define LDW1T(t, ks) wa[t][ks] = ld4(rw, ((t) < 2 ? l * 16 : vw3) + ((ks) & 3) * 1024, OFF_W1F + (T0 + (t)) * 16384 + ((((ks) >> 2) + rot) & 3) * 4096);
  const unsigned vt = (l < 43 ? ((i * 4 + w) * 86 + l) * 128 : OOB);
  const unsigned va = (2 * T0 + (l < (three ? 6 : 4) ? l : 0)) * 4;
  unsigned auxv;
  fence();
  STAMP(7);
  {
    u32x4 zr[2][2], zi[2][2];
#pragma unroll
    for (int ks = 0; ks < 2; ++ks)
#pragma unroll
      for (int t = 0; t < 2; ++t) {
        LAS char* src = sm + L_TMP + 8192 + ((((ks ^ kw) * 4 + g) * 32) + 16 * t + c) * 16;
        zr[t][ks] = *(LAS u32x4*)src;
        zi[t][ks] = *(LAS u32x4*)(src + 4096);
      }
    const u32x4 sd = *(LAS u32x4*)(sm + L_TMP + 16384 + (32 * jc + 8 * g) * 2);
    const u32x4 wq0 = *(LAS u32x4*)(sm + L_TMP + 20480 + tid * 48), wq1 = *(LAS u32x4*)(sm + L_TMP + 20480 + tid * 48 + 16),
                wq2 = *(LAS u32x4*)(sm + L_TMP + 20480 + tid * 48 + 32);
    const u32x4 wf0 = {wq0[0], wq0[1], wq0[2], wq1[0]}, wf1 = {wq1[1], wq1[2], wq2[0], wq2[1]};
    u32x4 bre, bim;
    f32x4 hr[2], hi[2];
#pragma unroll
    for (int t = 0; t < 2; ++t) {
      f32x4 tr = mma16(zr[t][0], c1[0], z4);
      f32x4 ti = mma16(zi[t][0], c1[0], z4);
      tr = mma16(zi[t][0], s1[0], tr);
      ti = mma16(zr[t][0], neg4(s1[0]), ti);
      tr = mma16(zr[t][1], c1[1], tr);
      ti = mma16(zi[t][1], c1[1], ti);
      tr = mma16(zi[t][1], s1[1], tr);
      ti = mma16(zr[t][1], neg4(s1[1]), ti);
      float ur[4], ui[4];
#pragma unroll
      for (int r = 0; r < 4; ++r) {
        ur[r] = tr[r] * t1r[4 * t + r] - ti[r] * t1i[4 * t + r];
        ui[r] = tr[r] * t1i[4 * t + r] + ti[r] * t1r[4 * t + r];
      }
      bre[2 * t] = pk(ur[0], ur[1]); bre[2 * t + 1] = pk(ur[2], ur[3]);
      bim[2 * t] = pk(ui[0], ui[1]); bim[2 * t + 1] = pk(ui[2], ui[3]);
      const unsigned sa = sd[2 * t], sb = sd[2 * t + 1];
      const f32x4 sv = {lo16(sa), hi16(sa), lo16(sb), hi16(sb)};
      hr[t] = mma16(zr[t][0], sel, z4) * sv;
      hi[t] = mma16(zi[t][0], sel, z4) * sv;
    }
    u32x4 are, aim;
#pragma unroll
    for (int t = 0; t < 2; ++t) {
      f32x4 xr = mma16(c2[t], bre, z4);
      f32x4 xi = mma16(c2[t], bim, z4);
      xr = mma16(s2[t], bim, xr);
      xi = mma16(neg4(s2[t]), bre, xi);
      const u32x4 wf = t ? wf1 : wf0;
      float ur[4], ui[4];
#pragma unroll
      for (int r = 0; r < 4; ++r) {
        const float a = lo16(wf[r]), bq = hi16(wf[r]);
        ur[r] = xr[r] * a - xi[r] * bq;
        ui[r] = xr[r] * bq + xi[r] * a;
      }
      are[2 * t] = pk(ur[0], ur[1]); are[2 * t + 1] = pk(ur[2], ur[3]);
      aim[2 * t] = pk(ui[0], ui[1]); aim[2 * t + 1] = pk(ui[2], ui[3]);
    }
#pragma unroll
    for (int t = 0; t < 2; ++t) {
      f32x4 vr = mma16(are, c2[t], z4);
      f32x4 vi = mma16(aim, c2[t], z4);
      vr = mma16(aim, neg4(s2[t]), vr);
      vi = mma16(are, s2[t], vi);
      float ur[4], ui[4];
#pragma unroll
      for (int r = 0; r < 4; ++r) {
        ur[r] = vr[r] * t2r[4 * t + r] - vi[r] * t2i[4 * t + r];
        ui[r] = vr[r] * t2i[4 * t + r] + vi[r] * t2r[4 * t + r];
      }
      *(LAS u32x4*)(sm + L_TMP + (w * 2 + t) * 1024 + l * 16) = u32x4{pk(ur[0], ur[1]), pk(ur[2], ur[3]), pk(ui[0], ui[1]), pk(ui[2], ui[3])};
    }
    drain();
    bar();
    st1(tag, rw, (has1 && tid < 8) ? OFF_AUX + tid * 1024 + j1 * 4 : ((b == 0 && tid == 8) ? OFF_E : OOB), 0);
    STAMP(19);
#pragma unroll
    for (int ks = 0; ks < 2; ++ks)
#pragma unroll
      for (int hh = 0; hh < 2; ++hh) {
        const int src = 2 * (ks ^ kw) + hh;
        const u32x4 br = {c1[ks][2 * hh], c1[ks][2 * hh + 1], s1[ks][2 * hh] ^ 0x80008000u, s1[ks][2 * hh + 1] ^ 0x80008000u};
        const u32x4 bi = {s1[ks][2 * hh], s1[ks][2 * hh + 1], c1[ks][2 * hh], c1[ks][2 * hh + 1]};
#pragma unroll
        for (int t = 0; t < 2; ++t) {
          const u32x4 u = *(LAS u32x4*)(sm + L_TMP + (src * 2 + t) * 1024 + l * 16);
          hr[t] = mma16(u, br, hr[t]);
          hi[t] = mma16(u, bi, hi[t]);
        }
      }
    const u32x4 ore = {pk(hr[0][0], hr[0][1]), pk(hr[0][2], hr[0][3]), pk(hr[1][0], hr[1][1]), pk(hr[1][2], hr[1][3])};
    u32x4 oim = {pk(hi[0][0], hi[0][1]), pk(hi[0][2], hi[0][3]), pk(hi[1][0], hi[1][1]), pk(hi[1][2], hi[1][3])};
    if (pos == 255) oim = u32x4{0u, 0u, 0u, 0u};
    st4(clean(ore), rw, vh, OFF_H + kp * 128);
    st4(clean(oim), rw, vh + 128, OFF_H + kp * 128);
  }
  fence();
  tc0 = ld1(rw, vt, OFF_W1F); tc1 = ld1(rw, vt, OFF_W1F + 43 * 128);
  auxv = ld1(rw, va, OFF_AUX + xcd * 1024);
  fence();
  STAMP(8);
  const int n = l & 31, h = l >> 5, wk = (w + rot) & 3;
#pragma unroll
  for (int dd = 0; dd < 2; ++dd) {
    if (dd == 1) {
  if (any(auxv != tag)) {
#pragma unroll 1
    for (int spin = 0; spin < SPIN; ++spin) {
      asm volatile("" ::: "memory");
      if (!any(ld1(rw, va, OFF_AUX + xcd * 1024) != tag)) break;
    }
  }
    }
    const int d = 64 * cu + 32 * (dd ^ (w & 1)) + d_of_m(n);
    const float dl = (float)d * (1.f / 512);
    const float cd = cosr(dl), sn = sinr(dl);
    const f32x2 rc = {cd, cd}, rs = {sn, -sn};
    f32x2 p[2];
#pragma unroll
    for (int r = 0; r < 2; ++r) {
      const float a = (float)(((1 + 16 * h + 32 * r + 64 * wk) * d) & 511) * (1.f / 512);
      p[r] = f32x2{cosr(a) * 0.0625f, sinr(a) * -0.0625f};
    }
#pragma unroll
    for (int ii = 0; ii < 8; ++ii) {
      if (dd == 1 && ii < NB2) { fence(); LDW1(ii); fence(); }
      u32x4 f;
#pragma unroll
      for (int r = 0; r < 2; ++r) {
        f[2 * r] = pk(p[r][0], p[r][1]);
        p[r] = p[r] * rc + f32x2{p[r][1], p[r][0]} * rs;
        if (ii == 7 && r == 1) p[r][0] *= (wk == 3 && h == 1) ? 0.70710678118654752f : 1.f;
        f[2 * r + 1] = pk(p[r][0], p[r][1]);
        p[r] = p[r] * rc + f32x2{p[r][1], p[r][0]} * rs;
      }
      *(LAS u32x4*)(sm + L_BAS + w * 16384 + (2 * ii + dd) * 1024 + l * 16) = f;
    }
  }
  fence();
  STAMP(9);
  {
    const int row = tid >> 3, f = ((row >> 1) & 1) | (((row >> 3) & 1) << 1);
    LAS char* dst = sm + L_H + row * 128 + ((((tid & 7) >> 1) ^ f) * 32) + (tid & 1) * 16;
#pragma unroll 1
    for (int spin = 0; spin < SPIN; ++spin) {
      u32x4 hp[16];
      asm volatile("" ::: "memory");
#pragma unroll
      for (int it = 0; it < 16; ++it) hp[it] = ld4(rw, tid * 16, OFF_H + grp * 65536 + ((it + 4 * rot) & 15) * 4096);
      fence();
      unsigned m = 0;
#pragma unroll
      for (int it = 0; it < 16; ++it) {
        *(LAS u32x4*)(dst + it * 4096) = hp[it];
        m = umax(umax(m, umax(hp[it][0], hp[it][1])), umax(hp[it][2], hp[it][3]));
        fence();
      }
      if (!any(m == ~0u)) break;
    }
    STAMP(10);
    asm volatile("" :: "v"(tc0), "v"(tc1));
    tc0 = ld1(rw, vt, OFF_W2F); tc1 = ld1(rw, vt, OFF_W2F + 43 * 128);
    bar();
    STAMP(16);
  }
  fence();
  const int k0 = 22 * w;
  u32x4 fv[2][22];
  u32x2 e1[3];
#pragma unroll
  for (int t = 0; t < 3; ++t) e1[t] = __builtin_amdgcn_raw_buffer_load_b64(mk(b1, F_ * 2), (16 * (T0 + t) + 4 * g) * 2, 0, 0);
  const int rk = (5 * cu) >> 1;
  const unsigned vk = w < 3 ? l * 16 : OOB;
#define KOF(k) ((k) < 20 ? ((k) + rk >= 20 ? (k) + rk - 20 : (k) + rk) : (k))
#define LDW2T(dd, k) fv[dd][k] = ld4(rw, (k) < 20 ? l * 16 : vk, OFF_W2F + ((2 * cu + ((w & 1) ^ (dd))) * KS2 + k0 + KOF(k)) * 1024);
#define LDW2(k)                                                                                          \
  { const unsigned so_ = OFF_W2F + (2 * cu * KS2 + k0 + KOF(k)) * 1024;                                  \
    fv[0][k] = ld4(rw, (k) < 20 ? l * 16 : vk, so_ + (w & 1) * KS2 * 1024);                              \
    fv[1][k] = ld4(rw, (k) < 20 ? l * 16 : vk, so_ + ((w & 1) ^ 1) * KS2 * 1024); }
  fence();
  {
    f32x4 acc[3][4];
#pragma unroll
    for (int t = 0; t < 3; ++t)
#pragma unroll
      for (int st = 0; st < 4; ++st) acc[t][st] = z4;
    const int f = ((c >> 3) & 1) | ((g & 1) << 1);
    LAS char* rd[4];
#pragma unroll
    for (int st = 0; st < 4; ++st) rd[st] = sm + L_H + (8 * g + (c >> 2)) * 128 + ((st ^ f) * 32) + (c & 3) * 8;
    u32x4 hb[2][4];
#pragma unroll
    for (int st = 0; st < 4; ++st) {
      const u32x2 a0 = tr_read(rd[st]), a1 = tr_read(rd[st] + 512);
      hb[0][st] = u32x4{a0[0], a0[1], a1[0], a1[1]};
    }
#define SB __builtin_amdgcn_sched_barrier(0);
#define G1LOOP(GRAM)                                                                                     \
    _Pragma("unroll") for (int ks = 0; ks < 16; ++ks) {                                                  \
      if (GRAM && ks == 15) { _Pragma("unroll") for (int st = 0; st < 4; ++st) acc[2][st] = mma16(hb[1][st], hb[1][st], acc[2][st]); SB } \
      _Pragma("unroll") for (int st = 0; st < 4; ++st) {                                                 \
        u32x2 a0, a1;                                                                                    \
        acc[0][st] = mma16(wa[0][ks], hb[ks & 1][st], acc[0][st]);                                       \
        SB if (ks < 15) a0 = tr_read(rd[st] + (ks + 1) * 4096); SB                                       \
        acc[1][st] = mma16(wa[1][ks], hb[ks & 1][st], acc[1][st]);                                       \
        SB if (ks < 15) a1 = tr_read(rd[st] + (ks + 1) * 4096 + 512); SB                                 \
        if (!GRAM || ks < 15) acc[2][st] = mma16(GRAM ? hb[ks & 1][st] : wa[2][ks], hb[ks & 1][st], acc[2][st]); \
        else *(LAS f32x4*)(sm + L_TMP + st * 1024 + l * 16) = acc[2][st];                                \
        if (ks < 15) hb[(ks + 1) & 1][st] = u32x4{a0[0], a0[1], a1[0], a1[1]};                           \
        SB                                                                                               \
        if (NB2 + ks < 16) { if (st < 3) LDW1T(st, NB2 + ks) }                                           \
        else if (NB2 + ks - 16 < NW2) { if (st < 2) LDW2T(st, NB2 + ks - 16) }                           \
        SB                                                                                               \
      }                                                                                                  \
    }
    if (w == 3) {
      G1LOOP(true)
    } else {
      G1LOOP(false)
    }
    bar();
    STAMP(11);
    asm volatile("" :: "v"(tc0), "v"(tc1));
    float rstd[4];
#pragma unroll
    for (int st = 0; st < 4; ++st)
      rstd[st] = __builtin_amdgcn_rsqf(*(LAS float*)(sm + L_TMP + st * 1024 + ((c >> 2) * 16 + c) * 16 + (c & 3) * 4) * (1.f / D_) + EPS);
#pragma unroll
    for (int t = 0; t < 3; ++t) {
      const unsigned ea = e1[t][0], eb = e1[t][1];
      const float bias[4] = {lo16(ea), hi16(ea), lo16(eb), hi16(eb)};
      unsigned gp[4][2];
#pragma unroll
      for (int st = 0; st < 4; ++st) {
        float gv[4];
#pragma unroll
        for (int r = 0; r < 4; ++r) {
          const float pre = acc[t][st][r] * rstd[st] + bias[r];
          const float p = pre > 0.f ? pre : 0.f;
          gv[r] = p * p;
        }
        gp[st][0] = pk(gv[0], gv[1]); gp[st][1] = pk(gv[2], gv[3]);
      }
#pragma unroll
      for (int sp = 0; sp < 2; ++sp) {
        const auto a = __builtin_amdgcn_permlane16_swap(gp[2 * sp][0], gp[2 * sp + 1][0], false, false);
        const auto bq = __builtin_amdgcn_permlane16_swap(gp[2 * sp][1], gp[2 * sp + 1][1], false, false);
        st4(u32x4{a[0], bq[0], a[1], bq[1]}, rw, ((t < 2 || three) ? l * 16 : OOB) + sp * 1024, OFF_G + (grp * KS2 + T0 + t) * 2048);
      }
    }
  }
  fence();
  STAMP(17);
  f32x16 acc[4];
#pragma unroll
  for (int t = 0; t < 4; ++t)
#pragma unroll
    for (int r = 0; r < 16; ++r) acc[t][r] = 0.f;
  {
    const int qq = c >> 2;
    LAS char* rb = sm + L_H + (128 * w + (qq & 1) + 8 * (qq >> 1) + 4 * h) * 128 + (c & 3) * 8;
    LAS char* rd[2][2];
#pragma unroll
    for (int ss = 0; ss < 2; ++ss)
#pragma unroll
      for (int r = 0; r < 2; ++r) rd[ss][r] = rb + r * 256 + (((2 * (ss ^ (w >> 1)) + (g & 1)) ^ (r | (qq & 2))) * 32);
#pragma unroll
    for (int ii = 0; ii < 8; ++ii) {
      u32x4 hf[2], bf[2];
#pragma unroll
      for (int ss = 0; ss < 2; ++ss) {
        const u32x2 a0 = tr_read(rd[ss][0] + ii * 2048), a1 = tr_read(rd[ss][1] + ii * 2048);
        hf[ss] = u32x4{a0[0], a0[1], a1[0], a1[1]};
        bf[ss] = *(LAS u32x4*)(sm + L_BAS + w * 16384 + (2 * ii + ss) * 1024 + l * 16);
      }
      if (NW2 + ii < 22) LDW2(NW2 + ii);
      __builtin_amdgcn_sched_barrier(0);
      acc[0] = mma32(bf[0], hf[0], acc[0]);
      acc[2] = mma32(bf[0], hf[1], acc[2]);
      acc[1] = mma32(bf[1], hf[0], acc[1]);
      acc[3] = mma32(bf[1], hf[1], acc[3]);
      __builtin_amdgcn_sched_barrier(0);
    }
  }
  fence();
  waitvm<2 * (22 - NW2 < 8 ? 22 - NW2 : 8)>();
  st1(tag, rw, l < 8 ? l * 128 + (4 * cu + w) * 4 : OOB, OFF_F3 + grp * 1024);
  tc0 = ld1(rw, l < (three ? 48 : 32) ? l * 128 : OOB, OFF_G + (grp * KS2 + T0) * 2048);
  STAMP(12);
#pragma unroll
  for (int k = NW2 + 8; k < 22; ++k) LDW2(k);
  const int srow = 64 * grp + 32 * (w >> 1) + n, dcol = 64 * cu + 32 * (w & 1) + 16 * h;
  const unsigned mu = ld1(rw, srow * 4, OFF_MEAN);
  const u32x4 e20 = in4(mk(b2, D_ * 2), dcol * 2, 0), e21 = in4(mk(b2, D_ * 2), dcol * 2 + 16, 0);
  for (int spin = 0; spin < SPIN; ++spin) {
    asm volatile("" ::: "memory");
    const unsigned fl = ld1(rw, (8 * w + (l & 7)) * 4, OFF_F3 + (grp * 8 + cu) * 128);
    if (!any(fl != tag)) break;
  }
  fence();
  asm volatile("" :: "v"(tc0));
  STAMP(13);
  {
    u32x4 fg[2][22];
    const unsigned vg[2] = {l * 16u + kw * 1024u, l * 16u + (kw ^ 1) * 1024u};
#define LDG(ss, k) fg[ss][k] = ld4(rw, ((k) < 20 || w < 3) ? vg[ss] : OOB, OFF_G + (grp * KS2 + k0 + KOF(k)) * 2048);
#pragma unroll
    for (int k = 0; k < GD; ++k) { LDG(0, k) LDG(1, k) }
    fence();
#pragma unroll
    for (int k = 0; k < 22; ++k) {
      acc[0] = mma32(fv[0][k], fg[0][k], acc[0]);
      SB if (k + GD < 22) LDG(0, k + GD) SB
      acc[1] = mma32(fv[1][k], fg[0][k], acc[1]);
      SB
      acc[2] = mma32(fv[0][k], fg[1][k], acc[2]);
      SB if (k + GD < 22) LDG(1, k + GD) SB
      acc[3] = mma32(fv[1][k], fg[1][k], acc[3]);
      SB
    }
  }
  STAMP(14);
#pragma unroll
  for (int j = 1; j < 4; ++j)
#pragma unroll
    for (int qd = 0; qd < 4; ++qd)
      *(LAS f32x4*)(sm + L_BAS + w * 16384 + ((j - 1) * 4 + qd) * 1024 + l * 16) =
          f32x4{acc[j][4 * qd], acc[j][4 * qd + 1], acc[j][4 * qd + 2], acc[j][4 * qd + 3]};
  bar();
  float o[16];
  const float muf = __builtin_bit_cast(float, mu);
#pragma unroll
  for (int qd = 0; qd < 4; ++qd) {
    const f32x4 p1 = *(LAS f32x4*)(sm + L_BAS + (w ^ 1) * 16384 + (0 + qd) * 1024 + l * 16);
    const f32x4 p2 = *(LAS f32x4*)(sm + L_BAS + (w ^ 2) * 16384 + (4 + qd) * 1024 + l * 16);
    const f32x4 p3 = *(LAS f32x4*)(sm + L_BAS + (w ^ 3) * 16384 + (8 + qd) * 1024 + l * 16);
#pragma unroll
    for (int r = 0; r < 4; ++r) o[4 * qd + r] = (acc[0][4 * qd + r] + p1[r]) + (p2[r] + p3[r]) + muf;
  }
#pragma unroll
  for (int e = 0; e < 4; ++e) {
    o[2 * e] += lo16(e20[e]); o[2 * e + 1] += hi16(e20[e]);
    o[8 + 2 * e] += lo16(e21[e]); o[8 + 2 * e + 1] += hi16(e21[e]);
  }
  u16* dst = out + (size_t)srow * D_ + dcol;
  *(u32x4*)dst = u32x4{pk(o[0], o[1]), pk(o[2], o[3]), pk(o[4], o[5]), pk(o[6], o[7])};
  *(u32x4*)(dst + 8) = u32x4{pk(o[8], o[9]), pk(o[10], o[11]), pk(o[12], o[13]), pk(o[14], o[15])};
  STAMP(15);
  STAMP_FLUSH
}

extern "C" void kernel_launch(void* const* d_in, const int* in_sizes, int n_in, void* d_out, int out_size, void* d_ws,
                              size_t ws_size, hipStream_t stream) {
  fused<<<256, 256, 0, stream>>>((const u16*)d_in[0], (const u16*)d_in[1], (const u16*)d_in[2], (const u16*)d_in[4], (const u16*)d_in[5],
                                 (const u16*)d_in[6], (const u16*)d_in[7], (char*)d_ws, (u16*)d_out);
}
```

```cpp
#include <hip/hip_runtime.h>
#include <hip/hip_bf16.h>
#include <stdint.h>

typedef __attribute__((ext_vector_type(8))) __bf16 bf16x8;
typedef __attribute__((ext_vector_type(2))) __bf16 bf16x2;
typedef __attribute__((ext_vector_type(4))) short s16x4;
typedef __attribute__((ext_vector_type(4))) float f32x4;
typedef __attribute__((ext_vector_type(16))) float f32x16;
typedef __attribute__((ext_vector_type(4))) unsigned u32x4;
typedef __attribute__((ext_vector_type(2))) unsigned u32x2;
typedef unsigned short u16;
#define LAS __attribute__((address_space(3)))
#define DEV __device__ __forceinline__

typedef __attribute__((ext_vector_type(2))) float f32x2;
#define STAMP_DECL
#define STAMP(i)
#define STAMP_FLUSH

constexpr int S_ = 2048, D_ = 512, F_ = 1376, DF_ = 257;
constexpr int KS2 = F_ / 16;
constexpr float EPS = 1e-5f;
constexpr int SPIN = 1 << 16;
#define GD 8
#define NW2 4
#define NA 0
#define NB2 6

constexpr unsigned ZSTR = 16384 + 256, WSTR = 12288 + 256;
constexpr unsigned OFF_E = 0;
constexpr unsigned OFF_AUX = 1024;
constexpr unsigned OFF_F3 = 16384;
constexpr unsigned OFF_MEAN = OFF_F3 + 32768;
constexpr unsigned OFF_Z = 65536;
constexpr unsigned OFF_W = OFF_Z + 256 * ZSTR;
constexpr unsigned OFF_H = OFF_W + 256 * WSTR;
constexpr unsigned OFF_W1F = OFF_H + 2097152;
constexpr unsigned OFF_W2F = OFF_W1F + KS2 * 16384;
constexpr unsigned OFF_G = OFF_W2F + 16 * KS2 * 1024;
constexpr unsigned OFF_END = OFF_G + 32 * KS2 * 2048;

constexpr int L_H = 0, L_BAS = 65536, L_TMP = 131072, L_END = 163840;

DEV unsigned pk(float a, float b) {
  bf16x2 v; v.x = (__bf16)a; v.y = (__bf16)b;
  return __builtin_bit_cast(unsigned, v);
}
DEV float lo16(unsigned u) { return __builtin_bit_cast(float, u << 16); }
DEV float hi16(unsigned u) { return __builtin_bit_cast(float, u & 0xffff0000u); }
DEV float cosr(float rev) { return __builtin_amdgcn_cosf(rev); }
DEV float sinr(float rev) { return __builtin_amdgcn_sinf(rev); }
DEV f32x4 mma16(u32x4 a, u32x4 b, f32x4 c) {
  return __builtin_amdgcn_mfma_f32_16x16x32_bf16(__builtin_bit_cast(bf16x8, a), __builtin_bit_cast(bf16x8, b), c, 0, 0, 0);
}
DEV f32x16 mma32(u32x4 a, u32x4 b, f32x16 c) {
  return __builtin_amdgcn_mfma_f32_32x32x16_bf16(__builtin_bit_cast(bf16x8, a), __builtin_bit_cast(bf16x8, b), c, 0, 0, 0);
}
DEV u32x2 tr_read(LAS char* p) {
  return __builtin_bit_cast(u32x2, __builtin_amdgcn_ds_read_tr16_b64_v4i16((LAS s16x4*)p));
}
DEV float sq2(unsigned u, float acc) {
  const bf16x2 v = __builtin_bit_cast(bf16x2, u);
  return __builtin_amdgcn_fdot2_f32_bf16(v, v, acc, false);
}
DEV u32x4 neg4(u32x4 v) { return v ^ 0x80008000u; }
template <int CTRL> DEV float dpp_add(float v) {
  return v + __builtin_bit_cast(float, __builtin_amdgcn_update_dpp(0, __builtin_bit_cast(int, v), CTRL, 0xf, 0xf, true));
}
DEV float wave_sum(float v) {
  v = dpp_add<0xB1>(v);
  v = dpp_add<0x4E>(v);
  v = dpp_add<0x141>(v);
  v = dpp_add<0x140>(v);
  const int i = __builtin_bit_cast(int, v);
  return __builtin_bit_cast(float, __builtin_amdgcn_readlane(i, 0)) + __builtin_bit_cast(float, __builtin_amdgcn_readlane(i, 16)) +
         (__builtin_bit_cast(float, __builtin_amdgcn_readlane(i, 32)) + __builtin_bit_cast(float, __builtin_amdgcn_readlane(i, 48)));
}

struct RowDft {
  u32x4 c1[2], s1[2], a2;
  float twr[8], twi[8];
};
DEV void rowdft_init(RowDft& q, int l) {
  const int c = l & 15, g = l >> 4;
  const float fj = (float)(1 + c), fg = (float)(4 * g);
  const unsigned odd = (c & 1) ? 0u : 0x80008000u;
#pragma unroll
  for (int jj = 0; jj < 2; ++jj) {
    const float r0 = (fg + (float)(2 * jj)) * fj * (1.f / 32), r1 = r0 + fj * (1.f / 32);
    const unsigned cc = pk(cosr(r0), cosr(r1)), ss = pk(sinr(-r0), sinr(-r1));
    q.c1[0][jj] = cc; q.c1[0][jj + 2] = cc ^ odd;
    q.s1[0][jj] = ss; q.s1[0][jj + 2] = ss ^ odd;
  }
  q.c1[1] = q.c1[0] ^ 0x80000000u;
  q.s1[1] = q.s1[0] ^ 0x80000000u;
#pragma unroll
  for (int t = 0; t < 2; ++t) {
    const float dj = (fj + (float)(16 * t)) * (1.f / 512);
#pragma unroll
    for (int r = 0; r < 4; ++r) {
      const float rv = (fg + (float)r) * dj;
      q.twr[4 * t + r] = cosr(rv);
      q.twi[4 * t + r] = sinr(-rv);
    }
  }
  const float f2 = (float)(c >> 1) * (1.f / 16), sh = (c & 1) ? 0.25f : 0.f;
#pragma unroll
  for (int jj = 0; jj < 2; ++jj) {
    const float r0 = (fg + (float)(2 * jj)) * f2 + sh, r1 = r0 + f2;
    q.a2[jj] = pk(cosr(r0) * (1.f / 16), cosr(r1) * (1.f / 16));
    q.a2[jj + 2] = pk(sinr(r0) * (1.f / 16), sinr(r1) * (1.f / 16));
  }
}
DEV void rowdft(const RowDft& q, u32x4 a, int l, float (&o)[8]) {
  const f32x4 z = {0.f, 0.f, 0.f, 0.f};
#pragma unroll
  for (int t = 0; t < 2; ++t) {
    const f32x4 tr = mma16(a, q.c1[t], z), ti = mma16(a, q.s1[t], z);
    float ur[4], ui[4];
#pragma unroll
    for (int r = 0; r < 4; ++r) {
      ur[r] = tr[r] * q.twr[4 * t + r] - ti[r] * q.twi[4 * t + r];
      ui[r] = tr[r] * q.twi[4 * t + r] + ti[r] * q.twr[4 * t + r];
    }
    const u32x4 b = {pk(ur[0], ur[1]), pk(ur[2], ur[3]), pk(ui[0], ui[1]), pk(ui[2], ui[3])};
    const f32x4 x = mma16(q.a2, b, z);
#pragma unroll
    for (int r = 0; r < 4; ++r) o[4 * t + r] = x[r];
  }
  o[6] *= (l == 63) ? 0.70710678118654752f : 1.f;
  o[7] = (l == 63) ? 0.f : o[7];
}

DEV int d_of_m(int m) { return 16 * ((m >> 2) & 1) + (m & 3) + 4 * (m >> 3); }

typedef __amdgpu_buffer_rsrc_t rsrc_t;
DEV u32x4 ld4(rsrc_t r, unsigned v, unsigned s) { return __builtin_amdgcn_raw_buffer_load_b128(r, v, s, 16); }
DEV unsigned ld1(rsrc_t r, unsigned v, unsigned s) { return __builtin_amdgcn_raw_buffer_load_b32(r, v, s, 16); }
DEV void st4(u32x4 d, rsrc_t r, unsigned v, unsigned s) { __builtin_amdgcn_raw_buffer_store_b128(d, r, v, s, 16); }
DEV void st1(unsigned d, rsrc_t r, unsigned v, unsigned s) { __builtin_amdgcn_raw_buffer_store_b32(d, r, v, s, 16); }
DEV rsrc_t mk(const void* p, unsigned n) { return __builtin_amdgcn_make_buffer_rsrc((void*)p, 0, n, 0x00020000); }
DEV u32x4 in4(rsrc_t r, unsigned v, unsigned s) { return __builtin_amdgcn_raw_buffer_load_b128(r, v, s, 0); }
constexpr unsigned OOB = 0x40000000u;
DEV void drain() { asm volatile("s_waitcnt vmcnt(0)" ::: "memory"); }
DEV void bar() { asm volatile("s_waitcnt lgkmcnt(0)\n\ts_barrier" ::: "memory"); }
DEV void fence() { asm volatile("" ::: "memory"); __builtin_amdgcn_sched_barrier(0); }
template <int N> DEV void waitvm() { asm volatile("s_waitcnt vmcnt(%0)" :: "i"(N) : "memory"); }
DEV void keep(u32x4& v) { asm volatile("" : "+v"(v)); }
DEV void keep(float& v) { asm volatile("" : "+v"(v)); }
DEV bool any(bool p) { return __builtin_amdgcn_ballot_w64(p) != 0; }
DEV unsigned umin(unsigned a, unsigned b) { return a < b ? a : b; }
DEV unsigned umax(unsigned a, unsigned b) { return a > b ? a : b; }
DEV u32x4 clean(u32x4 v) {
  return u32x4{umin(v[0], 0xfffffffeu), umin(v[1], 0xfffffffeu), umin(v[2], 0xfffffffeu), umin(v[3], 0xfffffffeu)};
}
DEV void sum2(float a, float b, float& sa, float& sb) {
  const auto q = __builtin_amdgcn_permlane32_swap(__builtin_bit_cast(unsigned, a), __builtin_bit_cast(unsigned, b), false, false);
  const unsigned q0 = q[0], q1 = q[1];
  float v = __builtin_bit_cast(float, q0) + __builtin_bit_cast(float, q1);
  v = dpp_add<0xB1>(v);
  v = dpp_add<0x4E>(v);
  v = dpp_add<0x141>(v);
  v = dpp_add<0x140>(v);
  const int u = __builtin_bit_cast(int, v);
  sa = __builtin_bit_cast(float, __builtin_amdgcn_readlane(u, 0)) + __builtin_bit_cast(float, __builtin_amdgcn_readlane(u, 16));
  sb = __builtin_bit_cast(float, __builtin_amdgcn_readlane(u, 32)) + __builtin_bit_cast(float, __builtin_amdgcn_readlane(u, 48));
}
DEV float xsum(float v) {
  const unsigned u = __builtin_bit_cast(unsigned, v);
  const auto a = __builtin_amdgcn_permlane16_swap(u, u, false, false);
  const unsigned a0 = a[0], a1 = a[1];
  const float s = __builtin_bit_cast(float, a0) + __builtin_bit_cast(float, a1);
  const unsigned su = __builtin_bit_cast(unsigned, s);
  const auto q = __builtin_amdgcn_permlane32_swap(su, su, false, false);
  const unsigned q0 = q[0], q1 = q[1];
  return __builtin_bit_cast(float, q0) + __builtin_bit_cast(float, q1);
}

__global__ __launch_bounds__(256, 1) void fused(const u16* __restrict__ x, const u16* __restrict__ wr, const u16* __restrict__ wi,
                                                const u16* __restrict__ w1, const u16* __restrict__ b1, const u16* __restrict__ w2,
                                                const u16* __restrict__ b2, char* ws, u16* __restrict__ out) {
  __shared__ __attribute__((aligned(16))) char smem[L_END];
  LAS char* sm = (LAS char*)smem;
  const int b = blockIdx.x, tid = threadIdx.x, l = tid & 63;
  const int w = __builtin_amdgcn_readfirstlane(tid >> 6);
  const int xcd = b & 7, i = b >> 3, pos = xcd * 32 + i;
  const int c = l & 15, g = l >> 4;
  const rsrc_t rw = __builtin_amdgcn_make_buffer_rsrc(ws, 0, OFF_END, 0x00020000);
  const f32x4 z4 = {0.f, 0.f, 0.f, 0.f};
  asm volatile("" :: "s"(x), "s"(wr), "s"(wi), "s"(w1), "s"(b1), "s"(w2), "s"(b2), "s"(ws), "s"(out));
  STAMP_DECL
  STAMP(0);
  const int s0 = 32 * (32 * (xcd >> 2) + 16 * (w >> 1) + 4 * (xcd & 3) + 2 * (w & 1)) + 8 * ((i & 15) >> 2) + 4 * (i >> 4) + (i & 3);
  const rsrc_t rx = mk(x, S_ * D_ * 2);
  const u32x4 xa = in4(rx, l * 16, s0 * (D_ * 2)), xb = in4(rx, l * 16, (s0 + 32) * (D_ * 2));
  const unsigned ep = ld1(rw, 0, OFF_E);
  fence();
  const int kp = 8 * ((pos & 15) + 16 * (pos >> 6)) + 4 * ((pos >> 4) & 1) + 2 * ((pos >> 5) & 1);
  const int jc = 16 * w + c;
  const unsigned vh = (jc >> 1) * 65536 + (jc & 1) * 64 + g * 16;
  st4(u32x4{~0u, ~0u, ~0u, ~0u}, rw, vh, OFF_H + kp * 128);
  st4(u32x4{~0u, ~0u, ~0u, ~0u}, rw, vh + 128, OFF_H + kp * 128);
  fence();
  const int j0 = 16 * (pos >> 6) + (pos & 15) + 512 * ((pos >> 4) & 3);
  u16 fr[8], fi[8];
  {
    const rsrc_t r0 = __builtin_amdgcn_make_buffer_rsrc((void*)wr, 0, S_ * DF_ * 2, 0x00020000);
    const rsrc_t r1 = __builtin_amdgcn_make_buffer_rsrc((void*)wi, 0, S_ * DF_ * 2, 0x00020000);
#pragma unroll
    for (int e = 0; e < 8; ++e) {
      fr[e] = __builtin_amdgcn_raw_buffer_load_b16(r0, 2 + 2 * tid, (j0 + 64 * e) * (DF_ * 2), 0);
      fi[e] = __builtin_amdgcn_raw_buffer_load_b16(r1, 2 + 2 * tid, (j0 + 64 * e) * (DF_ * 2), 0);
    }
  }
  fence();
  unsigned tp0, tp1, tp2;
  {
    const char* pc;
    asm volatile("s_getpc_b64 %0" : "=s"(pc));
    tp0 = __builtin_amdgcn_raw_buffer_load_b32(mk(pc, 26624), tid * 128, 0, 0);
    tp1 = ld1(rw, (w * 64 + l) * 65536 + OFF_W, 0);
    tp2 = __builtin_amdgcn_raw_buffer_load_b32(mk(out, S_ * D_ * 2), l < 32 ? l * 65536 : OOB, 0, 16);
  }
  const int j1 = xcd * 22 + i;
  const bool has1 = i < 22 && j1 < F_ / 8;
  const int j2 = 3 * pos + w;
  const bool has2 = w < 3 && j2 < KS2 * 8;
  u32x4 v0, v1, y0, y1;
  unsigned tc0, tc1;
  const rsrc_t r1 = mk(w1, D_ * F_ * 2);
  {
    const rsrc_t r2 = mk(w2, D_ * F_ * 2);
    tc0 = __builtin_amdgcn_raw_buffer_load_b32(r1, l < 16 ? (16 * i + 4 * w + (l >> 2)) * (F_ * 2) + xcd * 352 + umin((l & 3) * 128, 348) : OOB, 0, 0);
    const unsigned o2 = has2 ? (16 * (j2 >> 3) + (l >> 3)) * (D_ * 2) + (j2 & 7) * 128 + (l & 7) * 16 : OOB;
    y0 = in4(r2, o2, 0);
    y1 = in4(r2, o2, 8 * D_ * 2);
  }
  fence();
  RowDft q;
  rowdft_init(q, l);
  keep(q.c1[0]); keep(q.c1[1]); keep(q.s1[0]); keep(q.s1[1]); keep(q.a2);
#pragma unroll
  for (int e = 0; e < 8; ++e) { keep(q.twr[e]); keep(q.twi[e]); }
  fence();
  fence();
  const unsigned tag = __builtin_amdgcn_readfirstlane(ep) + 1u;
  {
    LAS char* mine = sm + L_TMP + w * 2048;
    *(LAS u32x4*)(mine + l * 16) = xa;
    *(LAS u32x4*)(mine + 1024 + l * 16) = xb;
    STAMP(1);
    float o[2][8];
#pragma unroll
    for (int rr = 0; rr < 2; ++rr) {
      const u32x2 a0 = tr_read(mine + rr * 1024 + l * 8), a1 = tr_read(mine + rr * 1024 + 512 + l * 8);
      const u32x4 a = {a0[0], a0[1], a1[0], a1[1]};
      rowdft(q, a, l, o[rr]);
    }
    float rs[2], sdv[2];
    {
      float e0 = 0.f, e1 = 0.f;
#pragma unroll
      for (int e = 0; e < 8; ++e) { e0 += o[0][e] * o[0][e]; e1 += o[1][e] * o[1][e]; }
      float t0, t1;
      sum2(e0, e1, t0, t1);
      const float va = t0 * (1.f / D_) + EPS, vb = t1 * (1.f / D_) + EPS;
      rs[0] = __builtin_amdgcn_rsqf(va); rs[1] = __builtin_amdgcn_rsqf(vb);
      sdv[0] = va * rs[0]; sdv[1] = vb * rs[1];
    }
    const unsigned sp = pk(sdv[0], sdv[1]);
#pragma unroll
    for (int e = 0; e < 4; ++e) {
      const int kk = c + 16 * (e >> 1) + 32 * (e & 1) + 64 * g;
      *(LAS u32x4*)(sm + L_H + kk * 64 + ((w ^ ((c >> 1) & 3)) * 16)) =
          u32x4{pk(o[0][2 * e] * rs[0], o[1][2 * e] * rs[1]), pk(o[0][2 * e + 1] * rs[0], o[1][2 * e + 1] * rs[1]), sp, tag};
    }
    waitvm<22>();
    bar();
    STAMP(2);
#pragma unroll
    for (int it = 0; it < 4; ++it) {
      const int p = tid + 256 * it;
      st4(*(LAS u32x4*)(sm + L_H + (p >> 2) * 64 + (((p & 3) ^ ((p >> 3) & 3)) * 16)), rw, tid * 16 + it * 4096, OFF_Z + pos * ZSTR);
    }
    fence();
    STAMP(3);
    unsigned fw[8];
#pragma unroll
    for (int e = 0; e < 8; ++e) fw[e] = (unsigned)fr[e] | ((unsigned)fi[e] << 16);
    *(LAS u32x4*)(sm + L_H + 16384 + tid * 48) = u32x4{fw[0], fw[1], fw[2], tag};
    *(LAS u32x4*)(sm + L_H + 16384 + tid * 48 + 16) = u32x4{fw[3], fw[4], fw[5], tag};
    *(LAS u32x4*)(sm + L_H + 16384 + tid * 48 + 32) = u32x4{fw[6], fw[7], 0u, tag};
#pragma unroll
    for (int it = 0; it < 3; ++it)
      st4(*(LAS u32x4*)(sm + L_H + 16384 + w * 3072 + l * 16 + it * 1024), rw, w * 3072 + l * 16 + it * 1024, OFF_W + pos * WSTR);
  }
  fence();
  asm volatile("" :: "v"(tc0), "v"(tp0), "v"(tp1), "v"(tp2));
  v0 = in4(r1, has1 ? tid * (F_ * 2) + j1 * 16 : OOB, 0);
  v1 = in4(r1, has1 ? tid * (F_ * 2) + j1 * 16 : OOB, 256 * F_ * 2);
  fence();
  const int kw = w >> 1;
  u32x4 c1[2], s1[2], c2[2], s2[2], sel;
  float t1r[8], t1i[8], t2r[8], t2i[8];
  const float fjc = (float)jc, fg = (float)g;
  {
    const float dj = fjc * (1.f / 64), n0 = 4.f * fg + (float)(32 * kw);
#pragma unroll
    for (int jj = 0; jj < 4; ++jj) {
      const float r0 = (n0 + (float)(16 * (jj >> 1) + ((2 * jj) & 3))) * dj, r1 = r0 + dj;
      c1[0][jj] = pk(cosr(r0), cosr(r1));
      s1[0][jj] = pk(sinr(r0), sinr(r1));
    }
    const unsigned odd = (c & 1) ? 0x80008000u : 0u;
    c1[1] = c1[0] ^ odd;
    s1[1] = s1[0] ^ odd;
  }
  keep(c1[0]); keep(c1[1]); keep(s1[0]); keep(s1[1]);
#pragma unroll
  for (int t = 0; t < 2; ++t) {
    const float sg = (float)(8 * (c >> 2) + 4 * t + (c & 3));
#pragma unroll
    for (int jj = 0; jj < 4; ++jj) {
      const float r0 = (8.f * fg + (float)(2 * jj)) * (sg * (1.f / 32)), r1 = r0 + sg * (1.f / 32);
      c2[t][jj] = pk(cosr(r0), cosr(r1));
      s2[t][jj] = pk(sinr(r0), sinr(r1));
    }
#pragma unroll
    for (int r = 0; r < 4; ++r) {
      const float a = (8.f * fg + (float)(4 * t + r)) * (fjc * (1.f / 2048));
      t1r[4 * t + r] = cosr(a);
      t1i[4 * t + r] = sinr(-a);
      const float bq = (4.f * fg + (float)(16 * w + r)) * (sg * (1.f / 2048));
      t2r[4 * t + r] = cosr(bq) * (1.f / 2048);
      t2i[4 * t + r] = sinr(bq) * (1.f / 2048);
    }
  }
#pragma unroll
  for (int jj = 0; jj < 4; ++jj) {
    const bool on = ((jj >> 1) == (w & 1)) && ((c >> 2) == g) && (((c & 3) >> 1) == (jj & 1));
    sel[jj] = on ? ((c & 1) ? 0x3f800000u : 0x00003f80u) : 0u;
  }
  keep(c2[0]); keep(c2[1]); keep(s2[0]); keep(s2[1]); keep(sel);
#pragma unroll
  for (int e = 0; e < 8; ++e) { keep(t1r[e]); keep(t1i[e]); keep(t2r[e]); keep(t2i[e]); }
  fence();
  STAMP(4);
  const int grp = 4 * xcd + (i >> 3), cu = i & 7;
  const int T0 = (cu < 6 ? 11 * cu : 10 * cu + 6) + ((cu >= 6 && w == 3) ? 8 : 3 * w);
  const bool three = w < 2 || (w == 2 && cu < 6);
  fence();
  {
    u32x4 zp[4], wp[3];
    const unsigned vz = (tid >> 2) * ZSTR + (tid & 3) * 16;
    unsigned vw[3];
#pragma unroll
    for (int it = 0; it < 3; ++it) { const unsigned p = tid + 256 * it, pp = p / 3u; vw[it] = pp * WSTR + (p - 3 * pp) * 16; }
#define POLL1                                                                                            \
    _Pragma("unroll") for (int it = 0; it < 4; ++it) zp[it] = ld4(rw, vz, OFF_Z + pos * 64 + it * 64 * ZSTR); \
    _Pragma("unroll") for (int it = 0; it < 3; ++it) wp[it] = ld4(rw, vw[it], OFF_W + pos * 48);
    POLL1
    fence();
  if (has1) {
    *(LAS u32x4*)(sm + L_BAS + tid * 16) = v0;
    *(LAS u32x4*)(sm + L_BAS + 4096 + tid * 16) = v1;
    bar();
    unsigned dw[8];
#pragma unroll
    for (int j = 0; j < 8; ++j) dw[j] = *(LAS unsigned*)(sm + L_BAS + (16 * (4 * g + (j & 3) + 16 * (j >> 2)) + c) * 16 + w * 4);
#pragma unroll
    for (int rr = 0; rr < 2; ++rr) {
      u32x4 a;
#pragma unroll
      for (int jj = 0; jj < 4; ++jj) a[jj] = __builtin_amdgcn_perm(dw[2 * jj + 1], dw[2 * jj], rr ? 0x07060302u : 0x05040100u);
      float o[8];
      rowdft(q, a, l, o);
      *(LAS u32x4*)(sm + L_BAS + 8192 + l * 128 + (((2 * w + rr) ^ (l & 7)) * 16)) =
          u32x4{pk(o[0], o[1]), pk(o[2], o[3]), pk(o[4], o[5]), pk(o[6], o[7])};
    }
    bar();
  }
#pragma unroll
  for (int it = 0; it < 2; ++it) {
    const int ll = (tid >> 3) + 32 * it;
    st4(*(LAS u32x4*)(sm + L_BAS + 8192 + ll * 128 + (((tid & 7) ^ (ll & 7)) * 16)), rw,
        has1 ? ll * 256 + (tid & 7) * 16 + (j1 >> 1) * 16384 + (j1 & 1) * 128 : OOB, OFF_W1F);
  }
  {
    LAS char* mine = sm + L_TMP + w * 2048;
    *(LAS u32x4*)(mine + l * 16) = y0;
    *(LAS u32x4*)(mine + 1024 + l * 16) = y1;
    LAS char* rd = mine + (8 * (g >> 1) + (c >> 2)) * 128 + (16 * (c & 1) + 8 * (g & 1) + 4 * ((c >> 1) & 1)) * 2;
#pragma unroll
    for (int dt = 0; dt < 2; ++dt) {
      const u32x2 a0 = tr_read(rd + dt * 64), a1 = tr_read(rd + dt * 64 + 512);
      st4(u32x4{a0[0], a0[1], a1[0], a1[1]}, rw, has2 ? l * 16 + ((2 * (j2 & 7) + dt) * KS2 + (j2 >> 3)) * 1024 : OOB, OFF_W2F);
    }
  }
    float m0 = 0.f, m1 = 0.f;
#pragma unroll
    for (int e = 0; e < 4; ++e) { m0 += lo16(xa[e]) + hi16(xa[e]); m1 += lo16(xb[e]) + hi16(xb[e]); }
    sum2(m0, m1, m0, m1);
    st1(__builtin_bit_cast(unsigned, (l ? m1 : m0) * (1.f / D_)), rw, l < 2 ? (s0 + 32 * l) * 4 : OOB, OFF_MEAN);
    fence();
    STAMP(5);
#define BAD1 any((((zp[0][3] ^ tag) | (zp[1][3] ^ tag)) | ((zp[2][3] ^ tag) | (zp[3][3] ^ tag)) | ((wp[0][3] ^ tag) | (wp[1][3] ^ tag)) | (wp[2][3] ^ tag)) != 0)
    if (BAD1) {
#pragma unroll 1
      for (int spin = 0; spin < SPIN; ++spin) {
        asm volatile("" ::: "memory");
        POLL1
        if (!BAD1) break;
      }
    }
    STAMP(6);
#pragma unroll
    for (int it = 0; it < 4; ++it) {
      const int pp = 64 * it + (tid >> 2), qq = tid & 3, px = pp >> 5, pi = pp & 31;
      *(LAS unsigned*)(sm + L_TMP + 8192 + it * 1024 + tid * 4) = zp[it][0];
      *(LAS unsigned*)(sm + L_TMP + 12288 + it * 1024 + tid * 4) = zp[it][1];
      const int s = 32 * (32 * (px >> 2) + 16 * (qq >> 1) + 4 * (px & 3) + 2 * (qq & 1)) + 8 * ((pi & 15) >> 2) + 4 * (pi >> 4) + (pi & 3);
      *(LAS u16*)(sm + L_TMP + 16384 + s * 2) = (u16)zp[it][2];
      *(LAS u16*)(sm + L_TMP + 16384 + s * 2 + 64) = (u16)(zp[it][2] >> 16);
    }
#pragma unroll
    for (int it = 0; it < 3; ++it) *(LAS u32x4*)(sm + L_TMP + 20480 + it * 4096 + tid * 16) = wp[it];
    bar();
    STAMP(18);
  }
  fence();
  u32x4 wa[3][16];
  const int rot = cu & 3;
  const unsigned vw3 = three ? l * 16 : OOB;
#define LDW1(ks)                                                                                         \
  { const unsigned so_ = OFF_W1F + T0 * 16384 + ((((ks) >> 2) + rot) & 3) * 4096;                        \
    wa[0][ks] = ld4(rw, l * 16 + ((ks) & 3) * 1024, so_);                                                \
    wa[1][ks] = ld4(rw, l * 16 + ((ks) & 3) * 1024, so_ + 16384);                                        \
    wa[2][ks] = ld4(rw, vw3 + ((ks) & 3) * 1024, so_ + 32768); }
#define LDW1T(t, ks) wa[t][ks] = ld4(rw, ((t) < 2 ? l * 16 : vw3) + ((ks) & 3) * 1024, OFF_W1F + (T0 + (t)) * 16384 + ((((ks) >> 2) + rot) & 3) * 4096);
  const unsigned vt = (l < 43 ? ((i * 4 + w) * 86 + l) * 128 : OOB);
  const unsigned va = (2 * T0 + (l < (three ? 6 : 4) ? l : 0)) * 4;
  unsigned auxv;
  fence();
  STAMP(7);
  {
    u32x4 zr[2][2], zi[2][2];
#pragma unroll
    for (int ks = 0; ks < 2; ++ks)
#pragma unroll
      for (int t = 0; t < 2; ++t) {
        LAS char* src = sm + L_TMP + 8192 + ((((ks ^ kw) * 4 + g) * 32) + 16 * t + c) * 16;
        zr[t][ks] = *(LAS u32x4*)src;
        zi[t][ks] = *(LAS u32x4*)(src + 4096);
      }
    const u32x4 sd = *(LAS u32x4*)(sm + L_TMP + 16384 + (32 * jc + 8 * g) * 2);
    const u32x4 wq0 = *(LAS u32x4*)(sm + L_TMP + 20480 + tid * 48), wq1 = *(LAS u32x4*)(sm + L_TMP + 20480 + tid * 48 + 16),
                wq2 = *(LAS u32x4*)(sm + L_TMP + 20480 + tid * 48 + 32);
    const u32x4 wf0 = {wq0[0], wq0[1], wq0[2], wq1[0]}, wf1 = {wq1[1], wq1[2], wq2[0], wq2[1]};
    u32x4 bre, bim;
    f32x4 hr[2], hi[2];
#pragma unroll
    for (int t = 0; t < 2; ++t) {
      f32x4 tr = mma16(zr[t][0], c1[0], z4);
      f32x4 ti = mma16(zi[t][0], c1[0], z4);
      tr = mma16(zi[t][0], s1[0], tr);
      ti = mma16(zr[t][0], neg4(s1[0]), ti);
      tr = mma16(zr[t][1], c1[1], tr);
      ti = mma16(zi[t][1], c1[1], ti);
      tr = mma16(zi[t][1], s1[1], tr);
      ti = mma16(zr[t][1], neg4(s1[1]), ti);
      float ur[4], ui[4];
#pragma unroll
      for (int r = 0; r < 4; ++r) {
        ur[r] = tr[r] * t1r[4 * t + r] - ti[r] * t1i[4 * t + r];
        ui[r] = tr[r] * t1i[4 * t + r] + ti[r] * t1r[4 * t + r];
      }
      bre[2 * t] = pk(ur[0], ur[1]); bre[2 * t + 1] = pk(ur[2], ur[3]);
      bim[2 * t] = pk(ui[0], ui[1]); bim[2 * t + 1] = pk(ui[2], ui[3]);
      const unsigned sa = sd[2 * t], sb = sd[2 * t + 1];
      const f32x4 sv = {lo16(sa), hi16(sa), lo16(sb), hi16(sb)};
      hr[t] = mma16(zr[t][0], sel, z4) * sv;
      hi[t] = mma16(zi[t][0], sel, z4) * sv;
    }
    u32x4 are, aim;
#pragma unroll
    for (int t = 0; t < 2; ++t) {
      f32x4 xr = mma16(c2[t], bre, z4);
      f32x4 xi = mma16(c2[t], bim, z4);
      xr = mma16(s2[t], bim, xr);
      xi = mma16(neg4(s2[t]), bre, xi);
      const u32x4 wf = t ? wf1 : wf0;
      float ur[4], ui[4];
#pragma unroll
      for (int r = 0; r < 4; ++r) {
        const float a = lo16(wf[r]), bq = hi16(wf[r]);
        ur[r] = xr[r] * a - xi[r] * bq;
        ui[r] = xr[r] * bq + xi[r] * a;
      }
      are[2 * t] = pk(ur[0], ur[1]); are[2 * t + 1] = pk(ur[2], ur[3]);
      aim[2 * t] = pk(ui[0], ui[1]); aim[2 * t + 1] = pk(ui[2], ui[3]);
    }
#pragma unroll
    for (int t = 0; t < 2; ++t) {
      f32x4 vr = mma16(are, c2[t], z4);
      f32x4 vi = mma16(aim, c2[t], z4);
      vr = mma16(aim, neg4(s2[t]), vr);
      vi = mma16(are, s2[t], vi);
      float ur[4], ui[4];
#pragma unroll
      for (int r = 0; r < 4; ++r) {
        ur[r] = vr[r] * t2r[4 * t + r] - vi[r] * t2i[4 * t + r];
        ui[r] = vr[r] * t2i[4 * t + r] + vi[r] * t2r[4 * t + r];
      }
      *(LAS u32x4*)(sm + L_TMP + (w * 2 + t) * 1024 + l * 16) = u32x4{pk(ur[0], ur[1]), pk(ur[2], ur[3]), pk(ui[0], ui[1]), pk(ui[2], ui[3])};
    }
    drain();
    bar();
    st1(tag, rw, (has1 && tid < 8) ? OFF_AUX + tid * 1024 + j1 * 4 : ((b == 0 && tid == 8) ? OFF_E : OOB), 0);
    STAMP(19);
#pragma unroll
    for (int ks = 0; ks < 2; ++ks)
#pragma unroll
      for (int hh = 0; hh < 2; ++hh) {
        const int src = 2 * (ks ^ kw) + hh;
        const u32x4 br = {c1[ks][2 * hh], c1[ks][2 * hh + 1], s1[ks][2 * hh] ^ 0x80008000u, s1[ks][2 * hh + 1] ^ 0x80008000u};
        const u32x4 bi = {s1[ks][2 * hh], s1[ks][2 * hh + 1], c1[ks][2 * hh], c1[ks][2 * hh + 1]};
#pragma unroll
        for (int t = 0; t < 2; ++t) {
          const u32x4 u = *(LAS u32x4*)(sm + L_TMP + (src * 2 + t) * 1024 + l * 16);
          hr[t] = mma16(u, br, hr[t]);
          hi[t] = mma16(u, bi, hi[t]);
        }
      }
    const u32x4 ore = {pk(hr[0][0], hr[0][1]), pk(hr[0][2], hr[0][3]), pk(hr[1][0], hr[1][1]), pk(hr[1][2], hr[1][3])};
    u32x4 oim = {pk(hi[0][0], hi[0][1]), pk(hi[0][2], hi[0][3]), pk(hi[1][0], hi[1][1]), pk(hi[1][2], hi[1][3])};
    if (pos == 255) oim = u32x4{0u, 0u, 0u, 0u};
    st4(clean(ore), rw, vh, OFF_H + kp * 128);
    st4(clean(oim), rw, vh + 128, OFF_H + kp * 128);
  }
  fence();
  tc0 = ld1(rw, vt, OFF_W1F); tc1 = ld1(rw, vt, OFF_W1F + 43 * 128);
  auxv = ld1(rw, va, OFF_AUX + xcd * 1024);
  fence();
  STAMP(8);
  const int n = l & 31, h = l >> 5, wk = (w + rot) & 3;
#pragma unroll
  for (int dd = 0; dd < 2; ++dd) {
    if (dd == 1) {
  if (any(auxv != tag)) {
#pragma unroll 1
    for (int spin = 0; spin < SPIN; ++spin) {
      asm volatile("" ::: "memory");
      if (!any(ld1(rw, va, OFF_AUX + xcd * 1024) != tag)) break;
    }
  }
    }
    const int d = 64 * cu + 32 * (dd ^ (w & 1)) + d_of_m(n);
    const float dl = (float)d * (1.f / 512);
    const float cd = cosr(dl), sn = sinr(dl);
    const f32x2 rc = {cd, cd}, rs = {sn, -sn};
    f32x2 p[2];
#pragma unroll
    for (int r = 0; r < 2; ++r) {
      const float a = (float)(((1 + 16 * h + 32 * r + 64 * wk) * d) & 511) * (1.f / 512);
      p[r] = f32x2{cosr(a) * 0.0625f, sinr(a) * -0.0625f};
    }
#pragma unroll
    for (int ii = 0; ii < 8; ++ii) {
      if (dd == 1 && ii < NB2) { fence(); LDW1(ii); fence(); }
      u32x4 f;
#pragma unroll
      for (int r = 0; r < 2; ++r) {
        f[2 * r] = pk(p[r][0], p[r][1]);
        p[r] = p[r] * rc + f32x2{p[r][1], p[r][0]} * rs;
        if (ii == 7 && r == 1) p[r][0] *= (wk == 3 && h == 1) ? 0.70710678118654752f : 1.f;
        f[2 * r + 1] = pk(p[r][0], p[r][1]);
        p[r] = p[r] * rc + f32x2{p[r][1], p[r][0]} * rs;
      }
      *(LAS u32x4*)(sm + L_BAS + w * 16384 + (2 * ii + dd) * 1024 + l * 16) = f;
    }
  }
  fence();
  STAMP(9);
  {
    const int row = tid >> 3, f = ((row >> 1) & 1) | (((row >> 3) & 1) << 1);
    LAS char* dst = sm + L_H + row * 128 + ((((tid & 7) >> 1) ^ f) * 32) + (tid & 1) * 16;
#pragma unroll 1
    for (int spin = 0; spin < SPIN; ++spin) {
      u32x4 hp[16];
      asm volatile("" ::: "memory");
#pragma unroll
      for (int it = 0; it < 16; ++it) hp[it] = ld4(rw, tid * 16, OFF_H + grp * 65536 + ((it + 4 * rot) & 15) * 4096);
      fence();
      unsigned m = 0;
#pragma unroll
      for (int it = 0; it < 16; ++it) {
        *(LAS u32x4*)(dst + it * 4096) = hp[it];
        m = umax(umax(m, umax(hp[it][0], hp[it][1])), umax(hp[it][2], hp[it][3]));
        fence();
      }
      if (!any(m == ~0u)) break;
    }
    STAMP(10);
    asm volatile("" :: "v"(tc0), "v"(tc1));
    tc0 = ld1(rw, vt, OFF_W2F); tc1 = ld1(rw, vt, OFF_W2F + 43 * 128);
    bar();
    STAMP(16);
  }
  fence();
  const int k0 = 22 * w;
  u32x4 fv[2][22];
  u32x2 e1[3];
#pragma unroll
  for (int t = 0; t < 3; ++t) e1[t] = __builtin_amdgcn_raw_buffer_load_b64(mk(b1, F_ * 2), (16 * (T0 + t) + 4 * g) * 2, 0, 0);
  const int rk = (5 * cu) >> 1;
  const unsigned vk = w < 3 ? l * 16 : OOB;
#define KOF(k) ((k) < 20 ? ((k) + rk >= 20 ? (k) + rk - 20 : (k) + rk) : (k))
#define LDW2T(dd, k) fv[dd][k] = ld4(rw, (k) < 20 ? l * 16 : vk, OFF_W2F + ((2 * cu + ((w & 1) ^ (dd))) * KS2 + k0 + KOF(k)) * 1024);
#define LDW2(k)                                                                                          \
  { const unsigned so_ = OFF_W2F + (2 * cu * KS2 + k0 + KOF(k)) * 1024;                                  \
    fv[0][k] = ld4(rw, (k) < 20 ? l * 16 : vk, so_ + (w & 1) * KS2 * 1024);                              \
    fv[1][k] = ld4(rw, (k) < 20 ? l * 16 : vk, so_ + ((w & 1) ^ 1) * KS2 * 1024); }
  fence();
  {
    f32x4 acc[3][4];
#pragma unroll
    for (int t = 0; t < 3; ++t)
#pragma unroll
      for (int st = 0; st < 4; ++st) acc[t][st] = z4;
    const int f = ((c >> 3) & 1) | ((g & 1) << 1);
    LAS char* rd[4];
#pragma unroll
    for (int st = 0; st < 4; ++st) rd[st] = sm + L_H + (8 * g + (c >> 2)) * 128 + ((st ^ f) * 32) + (c & 3) * 8;
    u32x4 hb[2][4];
#pragma unroll
    for (int st = 0; st < 4; ++st) {
      const u32x2 a0 = tr_read(rd[st]), a1 = tr_read(rd[st] + 512);
      hb[0][st] = u32x4{a0[0], a0[1], a1[0], a1[1]};
    }
#define SB __builtin_amdgcn_sched_barrier(0);
#define G1LOOP(GRAM)                                                                                     \
    _Pragma("unroll") for (int ks = 0; ks < 16; ++ks) {                                                  \
      if (GRAM && ks == 15) { _Pragma("unroll") for (int st = 0; st < 4; ++st) acc[2][st] = mma16(hb[1][st], hb[1][st], acc[2][st]); SB } \
      _Pragma("unroll") for (int st = 0; st < 4; ++st) {                                                 \
        u32x2 a0, a1;                                                                                    \
        acc[0][st] = mma16(wa[0][ks], hb[ks & 1][st], acc[0][st]);                                       \
        SB if (ks < 15) a0 = tr_read(rd[st] + (ks + 1) * 4096); SB                                       \
        acc[1][st] = mma16(wa[1][ks], hb[ks & 1][st], acc[1][st]);                                       \
        SB if (ks < 15) a1 = tr_read(rd[st] + (ks + 1) * 4096 + 512); SB                                 \
        if (!GRAM || ks < 15) acc[2][st] = mma16(GRAM ? hb[ks & 1][st] : wa[2][ks], hb[ks & 1][st], acc[2][st]); \
        else *(LAS f32x4*)(sm + L_TMP + st * 1024 + l * 16) = acc[2][st];                                \
        if (ks < 15) hb[(ks + 1) & 1][st] = u32x4{a0[0], a0[1], a1[0], a1[1]};                           \
        SB                                                                                               \
        if (NB2 + ks < 16) { if (st < 3) LDW1T(st, NB2 + ks) }                                           \
        else if (NB2 + ks - 16 < NW2) { if (st < 2) LDW2T(st, NB2 + ks - 16) }                           \
        SB                                                                                               \
      }                                                                                                  \
    }
    if (w == 3) {
      G1LOOP(true)
    } else {
      G1LOOP(false)
    }
    bar();
    STAMP(11);
    asm volatile("" :: "v"(tc0), "v"(tc1));
    float rstd[4];
#pragma unroll
    for (int st = 0; st < 4; ++st)
      rstd[st] = __builtin_amdgcn_rsqf(*(LAS float*)(sm + L_TMP + st * 1024 + ((c >> 2) * 16 + c) * 16 + (c & 3) * 4) * (1.f / D_) + EPS);
#pragma unroll
    for (int t = 0; t < 3; ++t) {
      const unsigned ea = e1[t][0], eb = e1[t][1];
      const float bias[4] = {lo16(ea), hi16(ea), lo16(eb), hi16(eb)};
      unsigned gp[4][2];
#pragma unroll
      for (int st = 0; st < 4; ++st) {
        float gv[4];
#pragma unroll
        for (int r = 0; r < 4; ++r) {
          const float pre = acc[t][st][r] * rstd[st] + bias[r];
          const float p = pre > 0.f ? pre : 0.f;
          gv[r] = p * p;
        }
        gp[st][0] = pk(gv[0], gv[1]); gp[st][1] = pk(gv[2], gv[3]);
      }
#pragma unroll
      for (int sp = 0; sp < 2; ++sp) {
        const auto a = __builtin_amdgcn_permlane16_swap(gp[2 * sp][0], gp[2 * sp + 1][0], false, false);
        const auto bq = __builtin_amdgcn_permlane16_swap(gp[2 * sp][1], gp[2 * sp + 1][1], false, false);
        st4(u32x4{a[0], bq[0], a[1], bq[1]}, rw, ((t < 2 || three) ? l * 16 : OOB) + sp * 1024, OFF_G + (grp * KS2 + T0 + t) * 2048);
      }
    }
  }
  fence();
  STAMP(17);
  f32x16 acc[4];
#pragma unroll
  for (int t = 0; t < 4; ++t)
#pragma unroll
    for (int r = 0; r < 16; ++r) acc[t][r] = 0.f;
  {
    const int qq = c >> 2;
    LAS char* rb = sm + L_H + (128 * w + (qq & 1) + 8 * (qq >> 1) + 4 * h) * 128 + (c & 3) * 8;
    LAS char* rd[2][2];
#pragma unroll
    for (int ss = 0; ss < 2; ++ss)
#pragma unroll
      for (int r = 0; r < 2; ++r) rd[ss][r] = rb + r * 256 + (((2 * (ss ^ (w >> 1)) + (g & 1)) ^ (r | (qq & 2))) * 32);
#pragma unroll
    for (int ii = 0; ii < 8; ++ii) {
      u32x4 hf[2], bf[2];
#pragma unroll
      for (int ss = 0; ss < 2; ++ss) {
        const u32x2 a0 = tr_read(rd[ss][0] + ii * 2048), a1 = tr_read(rd[ss][1] + ii * 2048);
        hf[ss] = u32x4{a0[0], a0[1], a1[0], a1[1]};
        bf[ss] = *(LAS u32x4*)(sm + L_BAS + w * 16384 + (2 * ii + ss) * 1024 + l * 16);
      }
      if (NW2 + ii < 22) LDW2(NW2 + ii);
      __builtin_amdgcn_sched_barrier(0);
      acc[0] = mma32(bf[0], hf[0], acc[0]);
      acc[2] = mma32(bf[0], hf[1], acc[2]);
      acc[1] = mma32(bf[1], hf[0], acc[1]);
      acc[3] = mma32(bf[1], hf[1], acc[3]);
      __builtin_amdgcn_sched_barrier(0);
    }
  }
  fence();
  waitvm<2 * (22 - NW2 < 8 ? 22 - NW2 : 8)>();
  st1(tag, rw, l < 8 ? l * 128 + (4 * cu + w) * 4 : OOB, OFF_F3 + grp * 1024);
  tc0 = ld1(rw, l < (three ? 48 : 32) ? l * 128 : OOB, OFF_G + (grp * KS2 + T0) * 2048);
  STAMP(12);
#pragma unroll
  for (int k = NW2 + 8; k < 22; ++k) LDW2(k);
  const int srow = 64 * grp + 32 * (w >> 1) + n, dcol = 64 * cu + 32 * (w & 1) + 16 * h;
  const unsigned mu = ld1(rw, srow * 4, OFF_MEAN);
  const u32x4 e20 = in4(mk(b2, D_ * 2), dcol * 2, 0), e21 = in4(mk(b2, D_ * 2), dcol * 2 + 16, 0);
  for (int spin = 0; spin < SPIN; ++spin) {
    asm volatile("" ::: "memory");
    const unsigned fl = ld1(rw, (8 * w + (l & 7)) * 4, OFF_F3 + (grp * 8 + cu) * 128);
    if (!any(fl != tag)) break;
  }
  fence();
  asm volatile("" :: "v"(tc0));
  STAMP(13);
  {
    u32x4 fg[2][22];
    const unsigned vg[2] = {l * 16u + kw * 1024u, l * 16u + (kw ^ 1) * 1024u};
#define LDG(ss, k) fg[ss][k] = ld4(rw, ((k) < 20 || w < 3) ? vg[ss] : OOB, OFF_G + (grp * KS2 + k0 + KOF(k)) * 2048);
#pragma unroll
    for (int k = 0; k < GD; ++k) { LDG(0, k) LDG(1, k) }
    fence();
#pragma unroll
    for (int k = 0; k < 22; ++k) {
      acc[0] = mma32(fv[0][k], fg[0][k], acc[0]);
      SB if (k + GD < 22) LDG(0, k + GD) SB
      acc[1] = mma32(fv[1][k], fg[0][k], acc[1]);
      SB
      acc[2] = mma32(fv[0][k], fg[1][k], acc[2]);
      SB if (k + GD < 22) LDG(1, k + GD) SB
      acc[3] = mma32(fv[1][k], fg[1][k], acc[3]);
      SB
    }
  }
  STAMP(14);
#pragma unroll
  for (int j = 1; j < 4; ++j)
#pragma unroll
    for (int qd = 0; qd < 4; ++qd)
      *(LAS f32x4*)(sm + L_BAS + w * 16384 + ((j - 1) * 4 + qd) * 1024 + l * 16) =
          f32x4{acc[j][4 * qd], acc[j][4 * qd + 1], acc[j][4 * qd + 2], acc[j][4 * qd + 3]};
  bar();
  float o[16];
  const float muf = __builtin_bit_cast(float, mu);
#pragma unroll
  for (int qd = 0; qd < 4; ++qd) {
    const f32x4 p1 = *(LAS f32x4*)(sm + L_BAS + (w ^ 1) * 16384 + (0 + qd) * 1024 + l * 16);
    const f32x4 p2 = *(LAS f32x4*)(sm + L_BAS + (w ^ 2) * 16384 + (4 + qd) * 1024 + l * 16);
    const f32x4 p3 = *(LAS f32x4*)(sm + L_BAS + (w ^ 3) * 16384 + (8 + qd) * 1024 + l * 16);
#pragma unroll
    for (int r = 0; r < 4; ++r) o[4 * qd + r] = (acc[0][4 * qd + r] + p1[r]) + (p2[r] + p3[r]) + muf;
  }
#pragma unroll
  for (int e = 0; e < 4; ++e) {
    o[2 * e] += lo16(e20[e]); o[2 * e + 1] += hi16(e20[e]);
    o[8 + 2 * e] += lo16(e21[e]); o[8 + 2 * e + 1] += hi16(e21[e]);
  }
  u16* dst = out + (size_t)srow * D_ + dcol;
  *(u32x4*)dst = u32x4{pk(o[0], o[1]), pk(o[2], o[3]), pk(o[4], o[5]), pk(o[6], o[7])};
  *(u32x4*)(dst + 8) = u32x4{pk(o[8], o[9]), pk(o[10], o[11]), pk(o[12], o[13]), pk(o[14], o[15])};
  STAMP(15);
  STAMP_FLUSH
}

extern "C" void kernel_launch(void* const* d_in, const int* in_sizes, int n_in, void* d_out, int out_size, void* d_ws,
                              size_t ws_size, hipStream_t stream) {
  fused<<<256, 256, 0, stream>>>((const u16*)d_in[0], (const u16*)d_in[1], (const u16*)d_in[2], (const u16*)d_in[4], (const u16*)d_in[5],
                                 (const u16*)d_in[6], (const u16*)d_in[7], (char*)d_ws, (u16*)d_out);
}
```

```cpp
#include <hip/hip_runtime.h>
#include <hip/hip_bf16.h>
#include <stdint.h>

typedef __attribute__((ext_vector_type(8))) __bf16 bf16x8;
typedef __attribute__((ext_vector_type(2))) __bf16 bf16x2;
typedef __attribute__((ext_vector_type(4))) short s16x4;
typedef __attribute__((ext_vector_type(4))) float f32x4;
typedef __attribute__((ext_vector_type(16))) float f32x16;
typedef __attribute__((ext_vector_type(4))) unsigned u32x4;
typedef __attribute__((ext_vector_type(2))) unsigned u32x2;
typedef unsigned short u16;
#define LAS __attribute__((address_space(3)))
#define DEV __device__ __forceinline__

typedef __attribute__((ext_vector_type(2))) float f32x2;
#define STAMP_DECL
#define STAMP(i)
#define STAMP_FLUSH

constexpr int S_ = 2048, D_ = 512, F_ = 1376, DF_ = 257;
constexpr int KS2 = F_ / 16;
constexpr float EPS = 1e-5f;
constexpr int SPIN = 1 << 16;
#define OAUX 16
#define GD 8
#define NW2 4
#define NA 0
#define NB2 6

constexpr unsigned ZSTR = 16384 + 256, WSTR = 12288 + 256;
constexpr unsigned OFF_E = 0;
constexpr unsigned OFF_AUX = 1024;
constexpr unsigned OFF_F3 = 16384;
constexpr unsigned OFF_MEAN = OFF_F3 + 32768;
constexpr unsigned OFF_Z = 65536;
constexpr unsigned OFF_W = OFF_Z + 256 * ZSTR;
constexpr unsigned OFF_H = OFF_W + 256 * WSTR;
constexpr unsigned OFF_W1F = OFF_H + 2097152;
constexpr unsigned OFF_W2F = OFF_W1F + KS2 * 16384;
constexpr unsigned OFF_G = OFF_W2F + 16 * KS2 * 1024;
constexpr unsigned OFF_END = OFF_G + 32 * KS2 * 2048;

constexpr int L_H = 0, L_BAS = 65536, L_TMP = 131072, L_END = 163840;

DEV unsigned pk(float a, float b) {
  bf16x2 v; v.x = (__bf16)a; v.y = (__bf16)b;
  return __builtin_bit_cast(unsigned, v);
}
DEV float lo16(unsigned u) { return __builtin_bit_cast(float, u << 16); }
DEV float hi16(unsigned u) { return __builtin_bit_cast(float, u & 0xffff0000u); }
DEV float cosr(float rev) { return __builtin_amdgcn_cosf(rev); }
DEV float sinr(float rev) { return __builtin_amdgcn_sinf(rev); }
DEV f32x4 mma16(u32x4 a, u32x4 b, f32x4 c) {
  return __builtin_amdgcn_mfma_f32_16x16x32_bf16(__builtin_bit_cast(bf16x8, a), __builtin_bit_cast(bf16x8, b), c, 0, 0, 0);
}
DEV f32x16 mma32(u32x4 a, u32x4 b, f32x16 c) {
  return __builtin_amdgcn_mfma_f32_32x32x16_bf16(__builtin_bit_cast(bf16x8, a), __builtin_bit_cast(bf16x8, b), c, 0, 0, 0);
}
DEV u32x2 tr_read(LAS char* p) {
  return __builtin_bit_cast(u32x2, __builtin_amdgcn_ds_read_tr16_b64_v4i16((LAS s16x4*)p));
}
DEV float sq2(unsigned u, float acc) {
  const bf16x2 v = __builtin_bit_cast(bf16x2, u);
  return __builtin_amdgcn_fdot2_f32_bf16(v, v, acc, false);
}
DEV u32x4 neg4(u32x4 v) { return v ^ 0x80008000u; }
template <int CTRL> DEV float dpp_add(float v) {
  return v + __builtin_bit_cast(float, __builtin_amdgcn_update_dpp(0, __builtin_bit_cast(int, v), CTRL, 0xf, 0xf, true));
}
DEV float wave_sum(float v) {
  v = dpp_add<0xB1>(v);
  v = dpp_add<0x4E>(v);
  v = dpp_add<0x141>(v);
  v = dpp_add<0x140>(v);
  const int i = __builtin_bit_cast(int, v);
  return __builtin_bit_cast(float, __builtin_amdgcn_readlane(i, 0)) + __builtin_bit_cast(float, __builtin_amdgcn_readlane(i, 16)) +
         (__builtin_bit_cast(float, __builtin_amdgcn_readlane(i, 32)) + __builtin_bit_cast(float, __builtin_amdgcn_readlane(i, 48)));
}

struct RowDft {
  u32x4 c1[2], s1[2], a2;
  float twr[8], twi[8];
};
DEV void rowdft_init(RowDft& q, int l) {
  const int c = l & 15, g = l >> 4;
  const float fj = (float)(1 + c), fg = (float)(4 * g);
  const unsigned odd = (c & 1) ? 0u : 0x80008000u;
#pragma unroll
  for (int jj = 0; jj < 2; ++jj) {
    const float r0 = (fg + (float)(2 * jj)) * fj * (1.f / 32), r1 = r0 + fj * (1.f / 32);
    const unsigned cc = pk(cosr(r0), cosr(r1)), ss = pk(sinr(-r0), sinr(-r1));
    q.c1[0][jj] = cc; q.c1[0][jj + 2] = cc ^ odd;
    q.s1[0][jj] = ss; q.s1[0][jj + 2] = ss ^ odd;
  }
  q.c1[1] = q.c1[0] ^ 0x80000000u;
  q.s1[1] = q.s1[0] ^ 0x80000000u;
#pragma unroll
  for (int t = 0; t < 2; ++t) {
    const float dj = (fj + (float)(16 * t)) * (1.f / 512);
#pragma unroll
    for (int r = 0; r < 4; ++r) {
      const float rv = (fg + (float)r) * dj;
      q.twr[4 * t + r] = cosr(rv);
      q.twi[4 * t + r] = sinr(-rv);
    }
  }
  const float f2 = (float)(c >> 1) * (1.f / 16), sh = (c & 1) ? 0.25f : 0.f;
#pragma unroll
  for (int jj = 0; jj < 2; ++jj) {
    const float r0 = (fg + (float)(2 * jj)) * f2 + sh, r1 = r0 + f2;
    q.a2[jj] = pk(cosr(r0) * (1.f / 16), cosr(r1) * (1.f / 16));
    q.a2[jj + 2] = pk(sinr(r0) * (1.f / 16), sinr(r1) * (1.f / 16));
  }
}
DEV void rowdft(const RowDft& q, u32x4 a, int l, float (&o)[8]) {
  const f32x4 z = {0.f, 0.f, 0.f, 0.f};
#pragma unroll
  for (int t = 0; t < 2; ++t) {
    const f32x4 tr = mma16(a, q.c1[t], z), ti = mma16(a, q.s1[t], z);
    float ur[4], ui[4];
#pragma unroll
    for (int r = 0; r < 4; ++r) {
      ur[r] = tr[r] * q.twr[4 * t + r] - ti[r] * q.twi[4 * t + r];
      ui[r] = tr[r] * q.twi[4 * t + r] + ti[r] * q.twr[4 * t + r];
    }
    const u32x4 b = {pk(ur[0], ur[1]), pk(ur[2], ur[3]), pk(ui[0], ui[1]), pk(ui[2], ui[3])};
    const f32x4 x = mma16(q.a2, b, z);
#pragma unroll
    for (int r = 0; r < 4; ++r) o[4 * t + r] = x[r];
  }
  o[6] *= (l == 63) ? 0.70710678118654752f : 1.f;
  o[7] = (l == 63) ? 0.f : o[7];
}

DEV int d_of_m(int m) { return 16 * ((m >> 2) & 1) + (m & 3) + 4 * (m >> 3); }

typedef __amdgpu_buffer_rsrc_t rsrc_t;
DEV u32x4 ld4(rsrc_t r, unsigned v, unsigned s) { return __builtin_amdgcn_raw_buffer_load_b128(r, v, s, 16); }
DEV unsigned ld1(rsrc_t r, unsigned v, unsigned s) { return __builtin_amdgcn_raw_buffer_load_b32(r, v, s, 16); }
DEV void st4(u32x4 d, rsrc_t r, unsigned v, unsigned s) { __builtin_amdgcn_raw_buffer_store_b128(d, r, v, s, 16); }
DEV void st1(unsigned d, rsrc_t r, unsigned v, unsigned s) { __builtin_amdgcn_raw_buffer_store_b32(d, r, v, s, 16); }
DEV rsrc_t mk(const void* p, unsigned n) { return __builtin_amdgcn_make_buffer_rsrc((void*)p, 0, n, 0x00020000); }
DEV u32x4 in4(rsrc_t r, unsigned v, unsigned s) { return __builtin_amdgcn_raw_buffer_load_b128(r, v, s, 0); }
constexpr unsigned OOB = 0x40000000u;
DEV void drain() { asm volatile("s_waitcnt vmcnt(0)" ::: "memory"); }
DEV void bar() { asm volatile("s_waitcnt lgkmcnt(0)\n\ts_barrier" ::: "memory"); }
DEV void fence() { asm volatile("" ::: "memory"); __builtin_amdgcn_sched_barrier(0); }
template <int N> DEV void waitvm() { asm volatile("s_waitcnt vmcnt(%0)" :: "i"(N) : "memory"); }
DEV void keep(u32x4& v) { asm volatile("" : "+v"(v)); }
DEV void keep(float& v) { asm volatile("" : "+v"(v)); }
DEV bool any(bool p) { return __builtin_amdgcn_ballot_w64(p) != 0; }
DEV unsigned umin(unsigned a, unsigned b) { return a < b ? a : b; }
DEV unsigned umax(unsigned a, unsigned b) { return a > b ? a : b; }
DEV u32x4 clean(u32x4 v) {
  return u32x4{umin(v[0], 0xfffffffeu), umin(v[1], 0xfffffffeu), umin(v[2], 0xfffffffeu), umin(v[3], 0xfffffffeu)};
}
DEV void sum2(float a, float b, float& sa, float& sb) {
  const auto q = __builtin_amdgcn_permlane32_swap(__builtin_bit_cast(unsigned, a), __builtin_bit_cast(unsigned, b), false, false);
  const unsigned q0 = q[0], q1 = q[1];
  float v = __builtin_bit_cast(float, q0) + __builtin_bit_cast(float, q1);
  v = dpp_add<0xB1>(v);
  v = dpp_add<0x4E>(v);
  v = dpp_add<0x141>(v);
  v = dpp_add<0x140>(v);
  const int u = __builtin_bit_cast(int, v);
  sa = __builtin_bit_cast(float, __builtin_amdgcn_readlane(u, 0)) + __builtin_bit_cast(float, __builtin_amdgcn_readlane(u, 16));
  sb = __builtin_bit_cast(float, __builtin_amdgcn_readlane(u, 32)) + __builtin_bit_cast(float, __builtin_amdgcn_readlane(u, 48));
}
DEV float xsum(float v) {
  const unsigned u = __builtin_bit_cast(unsigned, v);
  const auto a = __builtin_amdgcn_permlane16_swap(u, u, false, false);
  const unsigned a0 = a[0], a1 = a[1];
  const float s = __builtin_bit_cast(float, a0) + __builtin_bit_cast(float, a1);
  const unsigned su = __builtin_bit_cast(unsigned, s);
  const auto q = __builtin_amdgcn_permlane32_swap(su, su, false, false);
  const unsigned q0 = q[0], q1 = q[1];
  return __builtin_bit_cast(float, q0) + __builtin_bit_cast(float, q1);
}

__global__ __launch_bounds__(256, 1) void fused(const u16* __restrict__ x, const u16* __restrict__ wr, const u16* __restrict__ wi,
                                                const u16* __restrict__ w1, const u16* __restrict__ b1, const u16* __restrict__ w2,
                                                const u16* __restrict__ b2, char* ws, u16* __restrict__ out) {
  __shared__ __attribute__((aligned(16))) char smem[L_END];
  LAS char* sm = (LAS char*)smem;
  const int b = blockIdx.x, tid = threadIdx.x, l = tid & 63;
  const int w = __builtin_amdgcn_readfirstlane(tid >> 6);
  const int xcd = b & 7, i = b >> 3, pos = xcd * 32 + i;
  const int c = l & 15, g = l >> 4;
  const rsrc_t rw = __builtin_amdgcn_make_buffer_rsrc(ws, 0, OFF_END, 0x00020000);
  const f32x4 z4 = {0.f, 0.f, 0.f, 0.f};
  asm volatile("" :: "s"(x), "s"(wr), "s"(wi), "s"(w1), "s"(b1), "s"(w2), "s"(b2), "s"(ws), "s"(out));
  STAMP_DECL
  STAMP(0);
  const int s0 = 32 * (32 * (xcd >> 2) + 16 * (w >> 1) + 4 * (xcd & 3) + 2 * (w & 1)) + 8 * ((i & 15) >> 2) + 4 * (i >> 4) + (i & 3);
  const rsrc_t rx = mk(x, S_ * D_ * 2);
  const u32x4 xa = in4(rx, l * 16, s0 * (D_ * 2)), xb = in4(rx, l * 16, (s0 + 32) * (D_ * 2));
  const unsigned ep = ld1(rw, 0, OFF_E);
  fence();
  const int kp = 8 * ((pos & 15) + 16 * (pos >> 6)) + 4 * ((pos >> 4) & 1) + 2 * ((pos >> 5) & 1);
  const int jc = 16 * w + c;
  const unsigned vh = (jc >> 1) * 65536 + (jc & 1) * 64 + g * 16;
  st4(u32x4{~0u, ~0u, ~0u, ~0u}, rw, vh, OFF_H + kp * 128);
  st4(u32x4{~0u, ~0u, ~0u, ~0u}, rw, vh + 128, OFF_H + kp * 128);
  fence();
  const int j0 = 16 * (pos >> 6) + (pos & 15) + 512 * ((pos >> 4) & 3);
  u16 fr[8], fi[8];
  {
    const rsrc_t r0 = __builtin_amdgcn_make_buffer_rsrc((void*)wr, 0, S_ * DF_ * 2, 0x00020000);
    const rsrc_t r1 = __builtin_amdgcn_make_buffer_rsrc((void*)wi, 0, S_ * DF_ * 2, 0x00020000);
#pragma unroll
    for (int e = 0; e < 8; ++e) {
      fr[e] = __builtin_amdgcn_raw_buffer_load_b16(r0, 2 + 2 * tid, (j0 + 64 * e) * (DF_ * 2), 0);
      fi[e] = __builtin_amdgcn_raw_buffer_load_b16(r1, 2 + 2 * tid, (j0 + 64 * e) * (DF_ * 2), 0);
    }
  }
  fence();
  unsigned tp0, tp1, tp2;
  {
    const char* pc;
    asm volatile("s_getpc_b64 %0" : "=s"(pc));
    tp0 = __builtin_amdgcn_raw_buffer_load_b32(mk(pc, 26624), tid * 128, 0, 0);
    tp1 = ld1(rw, (w * 64 + l) * 65536 + OFF_W, 0);
    tp2 = __builtin_amdgcn_raw_buffer_load_b32(mk(out, S_ * D_ * 2), l < 32 ? l * 65536 : OOB, 0, 16);
  }
  const int j1 = xcd * 22 + i;
  const bool has1 = i < 22 && j1 < F_ / 8;
  const int j2 = 3 * pos + w;
  const bool has2 = w < 3 && j2 < KS2 * 8;
  u32x4 v0, v1, y0, y1;
  unsigned tc0, tc1;
  const rsrc_t r1 = mk(w1, D_ * F_ * 2);
  {
    const rsrc_t r2 = mk(w2, D_ * F_ * 2);
    tc0 = __builtin_amdgcn_raw_buffer_load_b32(r1, l < 16 ? (16 * i + 4 * w + (l >> 2)) * (F_ * 2) + xcd * 352 + umin((l & 3) * 128, 348) : OOB, 0, 0);
    const unsigned o2 = has2 ? (16 * (j2 >> 3) + (l >> 3)) * (D_ * 2) + (j2 & 7) * 128 + (l & 7) * 16 : OOB;
    y0 = in4(r2, o2, 0);
    y1 = in4(r2, o2, 8 * D_ * 2);
  }
  fence();
  RowDft q;
  rowdft_init(q, l);
  keep(q.c1[0]); keep(q.c1[1]); keep(q.s1[0]); keep(q.s1[1]); keep(q.a2);
#pragma unroll
  for (int e = 0; e < 8; ++e) { keep(q.twr[e]); keep(q.twi[e]); }
  fence();
  fence();
  const unsigned tag = __builtin_amdgcn_readfirstlane(ep) + 1u;
  {
    LAS char* mine = sm + L_TMP + w * 2048;
    *(LAS u32x4*)(mine + l * 16) = xa;
    *(LAS u32x4*)(mine + 1024 + l * 16) = xb;
    STAMP(1);
    float o[2][8];
#pragma unroll
    for (int rr = 0; rr < 2; ++rr) {
      const u32x2 a0 = tr_read(mine + rr * 1024 + l * 8), a1 = tr_read(mine + rr * 1024 + 512 + l * 8);
      const u32x4 a = {a0[0], a0[1], a1[0], a1[1]};
      rowdft(q, a, l, o[rr]);
    }
    float rs[2], sdv[2];
    {
      float e0 = 0.f, e1 = 0.f;
#pragma unroll
      for (int e = 0; e < 8; ++e) { e0 += o[0][e] * o[0][e]; e1 += o[1][e] * o[1][e]; }
      float t0, t1;
      sum2(e0, e1, t0, t1);
      const float va = t0 * (1.f / D_) + EPS, vb = t1 * (1.f / D_) + EPS;
      rs[0] = __builtin_amdgcn_rsqf(va); rs[1] = __builtin_amdgcn_rsqf(vb);
      sdv[0] = va * rs[0]; sdv[1] = vb * rs[1];
    }
    const unsigned sp = pk(sdv[0], sdv[1]);
#pragma unroll
    for (int e = 0; e < 4; ++e) {
      const int kk = c + 16 * (e >> 1) + 32 * (e & 1) + 64 * g;
      *(LAS u32x4*)(sm + L_H + kk * 64 + ((w ^ ((c >> 1) & 3)) * 16)) =
          u32x4{pk(o[0][2 * e] * rs[0], o[1][2 * e] * rs[1]), pk(o[0][2 * e + 1] * rs[0], o[1][2 * e + 1] * rs[1]), sp, tag};
    }
    waitvm<22>();
    bar();
    STAMP(2);
#pragma unroll
    for (int it = 0; it < 4; ++it) {
      const int p = tid + 256 * it;
      st4(*(LAS u32x4*)(sm + L_H + (p >> 2) * 64 + (((p & 3) ^ ((p >> 3) & 3)) * 16)), rw, tid * 16 + it * 4096, OFF_Z + pos * ZSTR);
    }
    fence();
    STAMP(3);
    unsigned fw[8];
#pragma unroll
    for (int e = 0; e < 8; ++e) fw[e] = (unsigned)fr[e] | ((unsigned)fi[e] << 16);
    *(LAS u32x4*)(sm + L_H + 16384 + tid * 48) = u32x4{fw[0], fw[1], fw[2], tag};
    *(LAS u32x4*)(sm + L_H + 16384 + tid * 48 + 16) = u32x4{fw[3], fw[4], fw[5], tag};
    *(LAS u32x4*)(sm + L_H + 16384 + tid * 48 + 32) = u32x4{fw[6], fw[7], 0u, tag};
#pragma unroll
    for (int it = 0; it < 3; ++it)
      st4(*(LAS u32x4*)(sm + L_H + 16384 + w * 3072 + l * 16 + it * 1024), rw, w * 3072 + l * 16 + it * 1024, OFF_W + pos * WSTR);
  }
  fence();
  asm volatile("" :: "v"(tc0), "v"(tp0), "v"(tp1), "v"(tp2));
  v0 = in4(r1, has1 ? tid * (F_ * 2) + j1 * 16 : OOB, 0);
  v1 = in4(r1, has1 ? tid * (F_ * 2) + j1 * 16 : OOB, 256 * F_ * 2);
  fence();
  const int kw = w >> 1;
  u32x4 c1[2], s1[2], c2[2], s2[2], sel;
  float t1r[8], t1i[8], t2r[8], t2i[8];
  const float fjc = (float)jc, fg = (float)g;
  {
    const float dj = fjc * (1.f / 64), n0 = 4.f * fg + (float)(32 * kw);
#pragma unroll
    for (int jj = 0; jj < 4; ++jj) {
      const float r0 = (n0 + (float)(16 * (jj >> 1) + ((2 * jj) & 3))) * dj, r1 = r0 + dj;
      c1[0][jj] = pk(cosr(r0), cosr(r1));
      s1[0][jj] = pk(sinr(r0), sinr(r1));
    }
    const unsigned odd = (c & 1) ? 0x80008000u : 0u;
    c1[1] = c1[0] ^ odd;
    s1[1] = s1[0] ^ odd;
  }
  keep(c1[0]); keep(c1[1]); keep(s1[0]); keep(s1[1]);
#pragma unroll
  for (int t = 0; t < 2; ++t) {
    const float sg = (float)(8 * (c >> 2) + 4 * t + (c & 3));
#pragma unroll
    for (int jj = 0; jj < 4; ++jj) {
      const float r0 = (8.f * fg + (float)(2 * jj)) * (sg * (1.f / 32)), r1 = r0 + sg * (1.f / 32);
      c2[t][jj] = pk(cosr(r0), cosr(r1));
      s2[t][jj] = pk(sinr(r0), sinr(r1));
    }
#pragma unroll
    for (int r = 0; r < 4; ++r) {
      const float a = (8.f * fg + (float)(4 * t + r)) * (fjc * (1.f / 2048));
      t1r[4 * t + r] = cosr(a);
      t1i[4 * t + r] = sinr(-a);
      const float bq = (4.f * fg + (float)(16 * w + r)) * (sg * (1.f / 2048));
      t2r[4 * t + r] = cosr(bq) * (1.f / 2048);
      t2i[4 * t + r] = sinr(bq) * (1.f / 2048);
    }
  }
#pragma unroll
  for (int jj = 0; jj < 4; ++jj) {
    const bool on = ((jj >> 1) == (w & 1)) && ((c >> 2) == g) && (((c & 3) >> 1) == (jj & 1));
    sel[jj] = on ? ((c & 1) ? 0x3f800000u : 0x00003f80u) : 0u;
  }
  keep(c2[0]); keep(c2[1]); keep(s2[0]); keep(s2[1]); keep(sel);
#pragma unroll
  for (int e = 0; e < 8; ++e) { keep(t1r[e]); keep(t1i[e]); keep(t2r[e]); keep(t2i[e]); }
  fence();
  STAMP(4);
  const int grp = 4 * xcd + (i >> 3), cu = i & 7;
  const int T0 = (cu < 6 ? 11 * cu : 10 * cu + 6) + ((cu >= 6 && w == 3) ? 8 : 3 * w);
  const bool three = w < 2 || (w == 2 && cu < 6);
  fence();
  {
    u32x4 zp[4], wp[3];
    const unsigned vz = (tid >> 2) * ZSTR + (tid & 3) * 16;
    unsigned vw[3];
#pragma unroll
    for (int it = 0; it < 3; ++it) { const unsigned p = tid + 256 * it, pp = p / 3u; vw[it] = pp * WSTR + (p - 3 * pp) * 16; }
#define POLL1                                                                                            \
    _Pragma("unroll") for (int it = 0; it < 4; ++it) zp[it] = ld4(rw, vz, OFF_Z + pos * 64 + it * 64 * ZSTR); \
    _Pragma("unroll") for (int it = 0; it < 3; ++it) wp[it] = ld4(rw, vw[it], OFF_W + pos * 48);
    POLL1
    fence();
  if (has1) {
    *(LAS u32x4*)(sm + L_BAS + tid * 16) = v0;
    *(LAS u32x4*)(sm + L_BAS + 4096 + tid * 16) = v1;
    bar();
    unsigned dw[8];
#pragma unroll
    for (int j = 0; j < 8; ++j) dw[j] = *(LAS unsigned*)(sm + L_BAS + (16 * (4 * g + (j & 3) + 16 * (j >> 2)) + c) * 16 + w * 4);
#pragma unroll
    for (int rr = 0; rr < 2; ++rr) {
      u32x4 a;
#pragma unroll
      for (int jj = 0; jj < 4; ++jj) a[jj] = __builtin_amdgcn_perm(dw[2 * jj + 1], dw[2 * jj], rr ? 0x07060302u : 0x05040100u);
      float o[8];
      rowdft(q, a, l, o);
      *(LAS u32x4*)(sm + L_BAS + 8192 + l * 128 + (((2 * w + rr) ^ (l & 7)) * 16)) =
          u32x4{pk(o[0], o[1]), pk(o[2], o[3]), pk(o[4], o[5]), pk(o[6], o[7])};
    }
    bar();
  }
#pragma unroll
  for (int it = 0; it < 2; ++it) {
    const int ll = (tid >> 3) + 32 * it;
    st4(*(LAS u32x4*)(sm + L_BAS + 8192 + ll * 128 + (((tid & 7) ^ (ll & 7)) * 16)), rw,
        has1 ? ll * 256 + (tid & 7) * 16 + (j1 >> 1) * 16384 + (j1 & 1) * 128 : OOB, OFF_W1F);
  }
  {
    LAS char* mine = sm + L_TMP + w * 2048;
    *(LAS u32x4*)(mine + l * 16) = y0;
    *(LAS u32x4*)(mine + 1024 + l * 16) = y1;
    LAS char* rd = mine + (8 * (g >> 1) + (c >> 2)) * 128 + (16 * (c & 1) + 8 * (g & 1) + 4 * ((c >> 1) & 1)) * 2;
#pragma unroll
    for (int dt = 0; dt < 2; ++dt) {
      const u32x2 a0 = tr_read(rd + dt * 64), a1 = tr_read(rd + dt * 64 + 512);
      st4(u32x4{a0[0], a0[1], a1[0], a1[1]}, rw, has2 ? l * 16 + ((2 * (j2 & 7) + dt) * KS2 + (j2 >> 3)) * 1024 : OOB, OFF_W2F);
    }
  }
    float m0 = 0.f, m1 = 0.f;
#pragma unroll
    for (int e = 0; e < 4; ++e) { m0 += lo16(xa[e]) + hi16(xa[e]); m1 += lo16(xb[e]) + hi16(xb[e]); }
    sum2(m0, m1, m0, m1);
    st1(__builtin_bit_cast(unsigned, (l ? m1 : m0) * (1.f / D_)), rw, l < 2 ? (s0 + 32 * l) * 4 : OOB, OFF_MEAN);
    fence();
    STAMP(5);
#define BAD1 any((((zp[0][3] ^ tag) | (zp[1][3] ^ tag)) | ((zp[2][3] ^ tag) | (zp[3][3] ^ tag)) | ((wp[0][3] ^ tag) | (wp[1][3] ^ tag)) | (wp[2][3] ^ tag)) != 0)
    if (BAD1) {
#pragma unroll 1
      for (int spin = 0; spin < SPIN; ++spin) {
        asm volatile("" ::: "memory");
        POLL1
        if (!BAD1) break;
      }
    }
    STAMP(6);
#pragma unroll
    for (int it = 0; it < 4; ++it) {
      const int pp = 64 * it + (tid >> 2), qq = tid & 3, px = pp >> 5, pi = pp & 31;
      *(LAS unsigned*)(sm + L_TMP + 8192 + it * 1024 + tid * 4) = zp[it][0];
      *(LAS unsigned*)(sm + L_TMP + 12288 + it * 1024 + tid * 4) = zp[it][1];
      const int s = 32 * (32 * (px >> 2) + 16 * (qq >> 1) + 4 * (px & 3) + 2 * (qq & 1)) + 8 * ((pi & 15) >> 2) + 4 * (pi >> 4) + (pi & 3);
      *(LAS u16*)(sm + L_TMP + 16384 + s * 2) = (u16)zp[it][2];
      *(LAS u16*)(sm + L_TMP + 16384 + s * 2 + 64) = (u16)(zp[it][2] >> 16);
    }
#pragma unroll
    for (int it = 0; it < 3; ++it) *(LAS u32x4*)(sm + L_TMP + 20480 + it * 4096 + tid * 16) = wp[it];
    bar();
    STAMP(18);
  }
  fence();
  u32x4 wa[3][16];
  const int rot = cu & 3;
  const unsigned vw3 = three ? l * 16 : OOB;
#define LDW1(ks)                                                                                         \
  { const unsigned so_ = OFF_W1F + T0 * 16384 + ((((ks) >> 2) + rot) & 3) * 4096;                        \
    wa[0][ks] = ld4(rw, l * 16 + ((ks) & 3) * 1024, so_);                                                \
    wa[1][ks] = ld4(rw, l * 16 + ((ks) & 3) * 1024, so_ + 16384);                                        \
    wa[2][ks] = ld4(rw, vw3 + ((ks) & 3) * 1024, so_ + 32768); }
#define LDW1T(t, ks) wa[t][ks] = ld4(rw, ((t) < 2 ? l * 16 : vw3) + ((ks) & 3) * 1024, OFF_W1F + (T0 + (t)) * 16384 + ((((ks) >> 2) + rot) & 3) * 4096);
  const unsigned vt = (l < 43 ? ((i * 4 + w) * 86 + l) * 128 : OOB);
  const unsigned va = (2 * T0 + (l < (three ? 6 : 4) ? l : 0)) * 4;
  unsigned auxv;
  fence();
  STAMP(7);
  {
    u32x4 zr[2][2], zi[2][2];
#pragma unroll
    for (int ks = 0; ks < 2; ++ks)
#pragma unroll
      for (int t = 0; t < 2; ++t) {
        LAS char* src = sm + L_TMP + 8192 + ((((ks ^ kw) * 4 + g) * 32) + 16 * t + c) * 16;
        zr[t][ks] = *(LAS u32x4*)src;
        zi[t][ks] = *(LAS u32x4*)(src + 4096);
      }
    const u32x4 sd = *(LAS u32x4*)(sm + L_TMP + 16384 + (32 * jc + 8 * g) * 2);
    const u32x4 wq0 = *(LAS u32x4*)(sm + L_TMP + 20480 + tid * 48), wq1 = *(LAS u32x4*)(sm + L_TMP + 20480 + tid * 48 + 16),
                wq2 = *(LAS u32x4*)(sm + L_TMP + 20480 + tid * 48 + 32);
    const u32x4 wf0 = {wq0[0], wq0[1], wq0[2], wq1[0]}, wf1 = {wq1[1], wq1[2], wq2[0], wq2[1]};
    u32x4 bre, bim;
    f32x4 hr[2], hi[2];
#pragma unroll
    for (int t = 0; t < 2; ++t) {
      f32x4 tr = mma16(zr[t][0], c1[0], z4);
      f32x4 ti = mma16(zi[t][0], c1[0], z4);
      tr = mma16(zi[t][0], s1[0], tr);
      ti = mma16(zr[t][0], neg4(s1[0]), ti);
      tr = mma16(zr[t][1], c1[1], tr);
      ti = mma16(zi[t][1], c1[1], ti);
      tr = mma16(zi[t][1], s1[1], tr);
      ti = mma16(zr[t][1], neg4(s1[1]), ti);
      float ur[4], ui[4];
#pragma unroll
      for (int r = 0; r < 4; ++r) {
        ur[r] = tr[r] * t1r[4 * t + r] - ti[r] * t1i[4 * t + r];
        ui[r] = tr[r] * t1i[4 * t + r] + ti[r] * t1r[4 * t + r];
      }
      bre[2 * t] = pk(ur[0], ur[1]); bre[2 * t + 1] = pk(ur[2], ur[3]);
      bim[2 * t] = pk(ui[0], ui[1]); bim[2 * t + 1] = pk(ui[2], ui[3]);
      const unsigned sa = sd[2 * t], sb = sd[2 * t + 1];
      const f32x4 sv = {lo16(sa), hi16(sa), lo16(sb), hi16(sb)};
      hr[t] = mma16(zr[t][0], sel, z4) * sv;
      hi[t] = mma16(zi[t][0], sel, z4) * sv;
    }
    u32x4 are, aim;
#pragma unroll
    for (int t = 0; t < 2; ++t) {
      f32x4 xr = mma16(c2[t], bre, z4);
      f32x4 xi = mma16(c2[t], bim, z4);
      xr = mma16(s2[t], bim, xr);
      xi = mma16(neg4(s2[t]), bre, xi);
      const u32x4 wf = t ? wf1 : wf0;
      float ur[4], ui[4];
#pragma unroll
      for (int r = 0; r < 4; ++r) {
        const float a = lo16(wf[r]), bq = hi16(wf[r]);
        ur[r] = xr[r] * a - xi[r] * bq;
        ui[r] = xr[r] * bq + xi[r] * a;
      }
      are[2 * t] = pk(ur[0], ur[1]); are[2 * t + 1] = pk(ur[2], ur[3]);
      aim[2 * t] = pk(ui[0], ui[1]); aim[2 * t + 1] = pk(ui[2], ui[3]);
    }
#pragma unroll
    for (int t = 0; t < 2; ++t) {
      f32x4 vr = mma16(are, c2[t], z4);
      f32x4 vi = mma16(aim, c2[t], z4);
      vr = mma16(aim, neg4(s2[t]), vr);
      vi = mma16(are, s2[t], vi);
      float ur[4], ui[4];
#pragma unroll
      for (int r = 0; r < 4; ++r) {
        ur[r] = vr[r] * t2r[4 * t + r] - vi[r] * t2i[4 * t + r];
        ui[r] = vr[r] * t2i[4 * t + r] + vi[r] * t2r[4 * t + r];
      }
      *(LAS u32x4*)(sm + L_TMP + (w * 2 + t) * 1024 + l * 16) = u32x4{pk(ur[0], ur[1]), pk(ur[2], ur[3]), pk(ui[0], ui[1]), pk(ui[2], ui[3])};
    }
    drain();
    bar();
    st1(tag, rw, (has1 && tid < 8) ? OFF_AUX + tid * 1024 + j1 * 4 : ((b == 0 && tid == 8) ? OFF_E : OOB), 0);
    STAMP(19);
#pragma unroll
    for (int ks = 0; ks < 2; ++ks)
#pragma unroll
      for (int hh = 0; hh < 2; ++hh) {
        const int src = 2 * (ks ^ kw) + hh;
        const u32x4 br = {c1[ks][2 * hh], c1[ks][2 * hh + 1], s1[ks][2 * hh] ^ 0x80008000u, s1[ks][2 * hh + 1] ^ 0x80008000u};
        const u32x4 bi = {s1[ks][2 * hh], s1[ks][2 * hh + 1], c1[ks][2 * hh], c1[ks][2 * hh + 1]};
#pragma unroll
        for (int t = 0; t < 2; ++t) {
          const u32x4 u = *(LAS u32x4*)(sm + L_TMP + (src * 2 + t) * 1024 + l * 16);
          hr[t] = mma16(u, br, hr[t]);
          hi[t] = mma16(u, bi, hi[t]);
        }
      }
    const u32x4 ore = {pk(hr[0][0], hr[0][1]), pk(hr[0][2], hr[0][3]), pk(hr[1][0], hr[1][1]), pk(hr[1][2], hr[1][3])};
    u32x4 oim = {pk(hi[0][0], hi[0][1]), pk(hi[0][2], hi[0][3]), pk(hi[1][0], hi[1][1]), pk(hi[1][2], hi[1][3])};
    if (pos == 255) oim = u32x4{0u, 0u, 0u, 0u};
    st4(clean(ore), rw, vh, OFF_H + kp * 128);
    st4(clean(oim), rw, vh + 128, OFF_H + kp * 128);
  }
  fence();
  tc0 = ld1(rw, vt, OFF_W1F); tc1 = ld1(rw, vt, OFF_W1F + 43 * 128);
  auxv = ld1(rw, va, OFF_AUX + xcd * 1024);
  fence();
  STAMP(8);
  const int n = l & 31, h = l >> 5, wk = (w + rot) & 3;
#pragma unroll
  for (int dd = 0; dd < 2; ++dd) {
    if (dd == 1) {
  if (any(auxv != tag)) {
#pragma unroll 1
    for (int spin = 0; spin < SPIN; ++spin) {
      asm volatile("" ::: "memory");
      if (!any(ld1(rw, va, OFF_AUX + xcd * 1024) != tag)) break;
    }
  }
    }
    const int d = 64 * cu + 32 * (dd ^ (w & 1)) + d_of_m(n);
    const float dl = (float)d * (1.f / 512);
    const float cd = cosr(dl), sn = sinr(dl);
    const f32x2 rc = {cd, cd}, rs = {sn, -sn};
    f32x2 p[2];
#pragma unroll
    for (int r = 0; r < 2; ++r) {
      const float a = (float)(((1 + 16 * h + 32 * r + 64 * wk) * d) & 511) * (1.f / 512);
      p[r] = f32x2{cosr(a) * 0.0625f, sinr(a) * -0.0625f};
    }
#pragma unroll
    for (int ii = 0; ii < 8; ++ii) {
      if (dd == 1 && ii < NB2) { fence(); LDW1(ii); fence(); }
      u32x4 f;
#pragma unroll
      for (int r = 0; r < 2; ++r) {
        f[2 * r] = pk(p[r][0], p[r][1]);
        p[r] = p[r] * rc + f32x2{p[r][1], p[r][0]} * rs;
        if (ii == 7 && r == 1) p[r][0] *= (wk == 3 && h == 1) ? 0.70710678118654752f : 1.f;
        f[2 * r + 1] = pk(p[r][0], p[r][1]);
        p[r] = p[r] * rc + f32x2{p[r][1], p[r][0]} * rs;
      }
      *(LAS u32x4*)(sm + L_BAS + w * 16384 + (2 * ii + dd) * 1024 + l * 16) = f;
    }
  }
  fence();
  STAMP(9);
  {
    const int row = tid >> 3, f = ((row >> 1) & 1) | (((row >> 3) & 1) << 1);
    LAS char* dst = sm + L_H + row * 128 + ((((tid & 7) >> 1) ^ f) * 32) + (tid & 1) * 16;
#pragma unroll 1
    for (int spin = 0; spin < SPIN; ++spin) {
      u32x4 hp[16];
      asm volatile("" ::: "memory");
#pragma unroll
      for (int it = 0; it < 16; ++it) hp[it] = ld4(rw, tid * 16, OFF_H + grp * 65536 + ((it + 4 * rot) & 15) * 4096);
      fence();
      unsigned m = 0;
#pragma unroll
      for (int it = 0; it < 16; ++it) {
        *(LAS u32x4*)(dst + it * 4096) = hp[it];
        m = umax(umax(m, umax(hp[it][0], hp[it][1])), umax(hp[it][2], hp[it][3]));
        fence();
      }
      if (!any(m == ~0u)) break;
    }
    STAMP(10);
    asm volatile("" :: "v"(tc0), "v"(tc1));
    tc0 = ld1(rw, vt, OFF_W2F); tc1 = ld1(rw, vt, OFF_W2F + 43 * 128);
    bar();
    STAMP(16);
  }
  fence();
  const int k0 = 22 * w;
  u32x4 fv[2][22];
  u32x2 e1[3];
#pragma unroll
  for (int t = 0; t < 3; ++t) e1[t] = __builtin_amdgcn_raw_buffer_load_b64(mk(b1, F_ * 2), (16 * (T0 + t) + 4 * g) * 2, 0, 0);
  const int rk = (5 * cu) >> 1;
  const unsigned vk = w < 3 ? l * 16 : OOB;
#define KOF(k) ((k) < 20 ? ((k) + rk >= 20 ? (k) + rk - 20 : (k) + rk) : (k))
#define LDW2T(dd, k) fv[dd][k] = ld4(rw, (k) < 20 ? l * 16 : vk, OFF_W2F + ((2 * cu + ((w & 1) ^ (dd))) * KS2 + k0 + KOF(k)) * 1024);
#define LDW2(k)                                                                                          \
  { const unsigned so_ = OFF_W2F + (2 * cu * KS2 + k0 + KOF(k)) * 1024;                                  \
    fv[0][k] = ld4(rw, (k) < 20 ? l * 16 : vk, so_ + (w & 1) * KS2 * 1024);                              \
    fv[1][k] = ld4(rw, (k) < 20 ? l * 16 : vk, so_ + ((w & 1) ^ 1) * KS2 * 1024); }
  fence();
  {
    f32x4 acc[3][4];
#pragma unroll
    for (int t = 0; t < 3; ++t)
#pragma unroll
      for (int st = 0; st < 4; ++st) acc[t][st] = z4;
    const int f = ((c >> 3) & 1) | ((g & 1) << 1);
    LAS char* rd[4];
#pragma unroll
    for (int st = 0; st < 4; ++st) rd[st] = sm + L_H + (8 * g + (c >> 2)) * 128 + ((st ^ f) * 32) + (c & 3) * 8;
    u32x4 hb[2][4];
#pragma unroll
    for (int st = 0; st < 4; ++st) {
      const u32x2 a0 = tr_read(rd[st]), a1 = tr_read(rd[st] + 512);
      hb[0][st] = u32x4{a0[0], a0[1], a1[0], a1[1]};
    }
#define SB __builtin_amdgcn_sched_barrier(0);
#define G1LOOP(GRAM)                                                                                     \
    _Pragma("unroll") for (int ks = 0; ks < 16; ++ks) {                                                  \
      if (GRAM && ks == 15) { _Pragma("unroll") for (int st = 0; st < 4; ++st) acc[2][st] = mma16(hb[1][st], hb[1][st], acc[2][st]); SB } \
      _Pragma("unroll") for (int st = 0; st < 4; ++st) {                                                 \
        u32x2 a0, a1;                                                                                    \
        acc[0][st] = mma16(wa[0][ks], hb[ks & 1][st], acc[0][st]);                                       \
        SB if (ks < 15) a0 = tr_read(rd[st] + (ks + 1) * 4096); SB                                       \
        acc[1][st] = mma16(wa[1][ks], hb[ks & 1][st], acc[1][st]);                                       \
        SB if (ks < 15) a1 = tr_read(rd[st] + (ks + 1) * 4096 + 512); SB                                 \
        if (!GRAM || ks < 15) acc[2][st] = mma16(GRAM ? hb[ks & 1][st] : wa[2][ks], hb[ks & 1][st], acc[2][st]); \
        else *(LAS f32x4*)(sm + L_TMP + st * 1024 + l * 16) = acc[2][st];                                \
        if (ks < 15) hb[(ks + 1) & 1][st] = u32x4{a0[0], a0[1], a1[0], a1[1]};                           \
        SB                                                                                               \
        if (NB2 + ks < 16) { if (st < 3) LDW1T(st, NB2 + ks) }                                           \
        else if (NB2 + ks - 16 < NW2) { if (st < 2) LDW2T(st, NB2 + ks - 16) }                           \
        SB                                                                                               \
      }                                                                                                  \
    }
    if (w == 3) {
      G1LOOP(true)
    } else {
      G1LOOP(false)
    }
    bar();
    STAMP(11);
    asm volatile("" :: "v"(tc0), "v"(tc1));
    float rstd[4];
#pragma unroll
    for (int st = 0; st < 4; ++st)
      rstd[st] = __builtin_amdgcn_rsqf(*(LAS float*)(sm + L_TMP + st * 1024 + ((c >> 2) * 16 + c) * 16 + (c & 3) * 4) * (1.f / D_) + EPS);
#pragma unroll
    for (int t = 0; t < 3; ++t) {
      const unsigned ea = e1[t][0], eb = e1[t][1];
      const float bias[4] = {lo16(ea), hi16(ea), lo16(eb), hi16(eb)};
      unsigned gp[4][2];
#pragma unroll
      for (int st = 0; st < 4; ++st) {
        float gv[4];
#pragma unroll
        for (int r = 0; r < 4; ++r) {
          const float pre = acc[t][st][r] * rstd[st] + bias[r];
          const float p = pre > 0.f ? pre : 0.f;
          gv[r] = p * p;
        }
        gp[st][0] = pk(gv[0], gv[1]); gp[st][1] = pk(gv[2], gv[3]);
      }
#pragma unroll
      for (int sp = 0; sp < 2; ++sp) {
        const auto a = __builtin_amdgcn_permlane16_swap(gp[2 * sp][0], gp[2 * sp + 1][0], false, false);
        const auto bq = __builtin_amdgcn_permlane16_swap(gp[2 * sp][1], gp[2 * sp + 1][1], false, false);
        st4(u32x4{a[0], bq[0], a[1], bq[1]}, rw, ((t < 2 || three) ? l * 16 : OOB) + sp * 1024, OFF_G + (grp * KS2 + T0 + t) * 2048);
      }
    }
  }
  fence();
  STAMP(17);
  f32x16 acc[4];
#pragma unroll
  for (int t = 0; t < 4; ++t)
#pragma unroll
    for (int r = 0; r < 16; ++r) acc[t][r] = 0.f;
  {
    const int qq = c >> 2;
    LAS char* rb = sm + L_H + (128 * w + (qq & 1) + 8 * (qq >> 1) + 4 * h) * 128 + (c & 3) * 8;
    LAS char* rd[2][2];
#pragma unroll
    for (int ss = 0; ss < 2; ++ss)
#pragma unroll
      for (int r = 0; r < 2; ++r) rd[ss][r] = rb + r * 256 + (((2 * (ss ^ (w >> 1)) + (g & 1)) ^ (r | (qq & 2))) * 32);
#pragma unroll
    for (int ii = 0; ii < 8; ++ii) {
      u32x4 hf[2], bf[2];
#pragma unroll
      for (int ss = 0; ss < 2; ++ss) {
        const u32x2 a0 = tr_read(rd[ss][0] + ii * 2048), a1 = tr_read(rd[ss][1] + ii * 2048);
        hf[ss] = u32x4{a0[0], a0[1], a1[0], a1[1]};
        bf[ss] = *(LAS u32x4*)(sm + L_BAS + w * 16384 + (2 * ii + ss) * 1024 + l * 16);
      }
      if (NW2 + ii < 22) LDW2(NW2 + ii);
      __builtin_amdgcn_sched_barrier(0);
      acc[0] = mma32(bf[0], hf[0], acc[0]);
      acc[2] = mma32(bf[0], hf[1], acc[2]);
      acc[1] = mma32(bf[1], hf[0], acc[1]);
      acc[3] = mma32(bf[1], hf[1], acc[3]);
      __builtin_amdgcn_sched_barrier(0);
    }
  }
  fence();
  waitvm<2 * (22 - NW2 < 8 ? 22 - NW2 : 8)>();
  st1(tag, rw, l < 8 ? l * 128 + (4 * cu + w) * 4 : OOB, OFF_F3 + grp * 1024);
  tc0 = ld1(rw, l < (three ? 48 : 32) ? l * 128 : OOB, OFF_G + (grp * KS2 + T0) * 2048);
  STAMP(12);
#pragma unroll
  for (int k = NW2 + 8; k < 22; ++k) LDW2(k);
  const int srow = 64 * grp + 32 * (w >> 1) + n, dcol = 64 * cu + 32 * (w & 1) + 16 * h;
  const unsigned mu = ld1(rw, srow * 4, OFF_MEAN);
  const u32x4 e20 = in4(mk(b2, D_ * 2), dcol * 2, 0), e21 = in4(mk(b2, D_ * 2), dcol * 2 + 16, 0);
  for (int spin = 0; spin < SPIN; ++spin) {
    asm volatile("" ::: "memory");
    const unsigned fl = ld1(rw, (8 * w + (l & 7)) * 4, OFF_F3 + (grp * 8 + cu) * 128);
    if (!any(fl != tag)) break;
  }
  fence();
  asm volatile("" :: "v"(tc0));
  STAMP(13);
  {
    u32x4 fg[2][22];
    const unsigned vg[2] = {l * 16u + kw * 1024u, l * 16u + (kw ^ 1) * 1024u};
#define LDG(ss, k) fg[ss][k] = ld4(rw, ((k) < 20 || w < 3) ? vg[ss] : OOB, OFF_G + (grp * KS2 + k0 + KOF(k)) * 2048);
#pragma unroll
    for (int k = 0; k < GD; ++k) { LDG(0, k) LDG(1, k) }
    fence();
#pragma unroll
    for (int k = 0; k < 22; ++k) {
      acc[0] = mma32(fv[0][k], fg[0][k], acc[0]);
      SB if (k + GD < 22) LDG(0, k + GD) SB
      acc[1] = mma32(fv[1][k], fg[0][k], acc[1]);
      SB
      acc[2] = mma32(fv[0][k], fg[1][k], acc[2]);
      SB if (k + GD < 22) LDG(1, k + GD) SB
      acc[3] = mma32(fv[1][k], fg[1][k], acc[3]);
      SB
    }
  }
  STAMP(14);
#pragma unroll
  for (int j = 1; j < 4; ++j)
#pragma unroll
    for (int qd = 0; qd < 4; ++qd)
      *(LAS f32x4*)(sm + L_BAS + w * 16384 + ((j - 1) * 4 + qd) * 1024 + l * 16) =
          f32x4{acc[j][4 * qd], acc[j][4 * qd + 1], acc[j][4 * qd + 2], acc[j][4 * qd + 3]};
  bar();
  float o[16];
  const float muf = __builtin_bit_cast(float, mu);
#pragma unroll
  for (int qd = 0; qd < 4; ++qd) {
    const f32x4 p1 = *(LAS f32x4*)(sm + L_BAS + (w ^ 1) * 16384 + (0 + qd) * 1024 + l * 16);
    const f32x4 p2 = *(LAS f32x4*)(sm + L_BAS + (w ^ 2) * 16384 + (4 + qd) * 1024 + l * 16);
    const f32x4 p3 = *(LAS f32x4*)(sm + L_BAS + (w ^ 3) * 16384 + (8 + qd) * 1024 + l * 16);
#pragma unroll
    for (int r = 0; r < 4; ++r) o[4 * qd + r] = (acc[0][4 * qd + r] + p1[r]) + (p2[r] + p3[r]) + muf;
  }
#pragma unroll
  for (int e = 0; e < 4; ++e) {
    o[2 * e] += lo16(e20[e]); o[2 * e + 1] += hi16(e20[e]);
    o[8 + 2 * e] += lo16(e21[e]); o[8 + 2 * e + 1] += hi16(e21[e]);
  }
  const rsrc_t ro = mk(out, S_ * D_ * 2);
  __builtin_amdgcn_raw_buffer_store_b128(u32x4{pk(o[0], o[1]), pk(o[2], o[3]), pk(o[4], o[5]), pk(o[6], o[7])}, ro, (srow * D_ + dcol) * 2, 0, OAUX);
  __builtin_amdgcn_raw_buffer_store_b128(u32x4{pk(o[8], o[9]), pk(o[10], o[11]), pk(o[12], o[13]), pk(o[14], o[15])}, ro, (srow * D_ + dcol) * 2 + 16, 0, OAUX);
  STAMP(15);
  STAMP_FLUSH
}

extern "C" void kernel_launch(void* const* d_in, const int* in_sizes, int n_in, void* d_out, int out_size, void* d_ws,
                              size_t ws_size, hipStream_t stream) {
  fused<<<256, 256, 0, stream>>>((const u16*)d_in[0], (const u16*)d_in[1], (const u16*)d_in[2], (const u16*)d_in[4], (const u16*)d_in[5],
                                 (const u16*)d_in[6], (const u16*)d_in[7], (char*)d_ws, (u16*)d_out);
}
```

```cpp
#include <hip/hip_runtime.h>
#include <hip/hip_bf16.h>
__global__ void k0(unsigned short* out, int n) {
    int i = blockIdx.x * blockDim.x + threadIdx.x;
    uint4 z = make_uint4(0,0,0,0);
    if (i * 16 < n) { ((uint4*)out)[2*i] = z; ((uint4*)out)[2*i+1] = z; }
}
extern "C" void kernel_launch(void* const* d_in, const int* in_sizes, int n_in,
                              void* d_out, int out_size, void* d_ws, size_t ws_size,
                              hipStream_t stream) {
    k0<<<256, 256, 0, stream>>>((unsigned short*)d_out, out_size);
}
```

```cpp
#define GAT 1
#define CPOS 2
#define JOBPOS 6
#define DRPOS 1
#define BASPOS 3
#define GPOS 2
#define TW2 0
#define XP 1
#define PZ 2
#define GD0 8
#define EF 1
#define TM 2
#define ML 1
#define OS 2
#define OAUX 0
#define OT 2
#define CODE 29632
#include <hip/hip_runtime.h>
#include <hip/hip_bf16.h>
#include <stdint.h>

typedef __attribute__((ext_vector_type(8))) __bf16 bf16x8;
typedef __attribute__((ext_vector_type(2))) __bf16 bf16x2;
typedef __attribute__((ext_vector_type(4))) short s16x4;
typedef __attribute__((ext_vector_type(4))) float f32x4;
typedef __attribute__((ext_vector_type(16))) float f32x16;
typedef __attribute__((ext_vector_type(4))) unsigned u32x4;
typedef __attribute__((ext_vector_type(2))) unsigned u32x2;
typedef unsigned short u16;
#define LAS __attribute__((address_space(3)))
#define DEV __device__ __forceinline__

typedef __attribute__((ext_vector_type(2))) float f32x2;
#define STAMP_DECL
#define STAMP(i)
#define STAMP_FLUSH

constexpr int S_ = 2048, D_ = 512, F_ = 1376, DF_ = 257;
constexpr int KS2 = F_ / 16;
constexpr float EPS = 1e-5f;
constexpr int SPIN = 1 << 16;
#define EB 0
#define EM 0
#define ES 0
#define EP 0
#define TB 0
#define FL 0
#define EK (22 - GD)
#define GD 8
#define NW2 20
#define FPOS 0
#define SL 4
#define GAUX 0
#define GF 0
#define NB2 8

constexpr unsigned ZSTR = 16384 + 12288 + 256;
constexpr unsigned OFF_E = 0;
constexpr unsigned OFF_AUX = 1024;
constexpr unsigned OFF_MEAN = OFF_AUX + 8 * 176 * 128;
constexpr unsigned OFF_F3 = OFF_MEAN + 1024 * 128;
constexpr unsigned OFF_Z = OFF_F3 + 32 * 8192;
constexpr unsigned OFF_W = OFF_Z + 16384;
constexpr unsigned OFF_H = OFF_Z + 256 * ZSTR;
constexpr unsigned OFF_W1F = OFF_H + 2097152;
constexpr unsigned OFF_W2F = OFF_W1F + KS2 * 16384;
constexpr unsigned OFF_G = OFF_W2F + 16 * KS2 * 1024;
constexpr unsigned OFF_END = OFF_G + 32 * KS2 * 2048;

constexpr int L_H = 0, L_BAS = 65536, L_TMP = 131072, L_END = 163840;

DEV unsigned pk(float a, float b) {
  bf16x2 v; v.x = (__bf16)a; v.y = (__bf16)b;
  return __builtin_bit_cast(unsigned, v);
}
DEV float lo16(unsigned u) { return __builtin_bit_cast(float, u << 16); }
DEV float hi16(unsigned u) { return __builtin_bit_cast(float, u & 0xffff0000u); }
DEV float cosr(float rev) { return __builtin_amdgcn_cosf(rev); }
DEV float sinr(float rev) { return __builtin_amdgcn_sinf(rev); }
DEV f32x4 mma16(u32x4 a, u32x4 b, f32x4 c) {
  return __builtin_amdgcn_mfma_f32_16x16x32_bf16(__builtin_bit_cast(bf16x8, a), __builtin_bit_cast(bf16x8, b), c, 0, 0, 0);
}
DEV f32x16 mma32(u32x4 a, u32x4 b, f32x16 c) {
  return __builtin_amdgcn_mfma_f32_32x32x16_bf16(__builtin_bit_cast(bf16x8, a), __builtin_bit_cast(bf16x8, b), c, 0, 0, 0);
}
DEV u32x2 tr_read(LAS char* p) {
  return __builtin_bit_cast(u32x2, __builtin_amdgcn_ds_read_tr16_b64_v4i16((LAS s16x4*)p));
}
DEV float sum2b(unsigned u, float acc) {
  return __builtin_amdgcn_fdot2_f32_bf16(__builtin_bit_cast(bf16x2, u), __builtin_bit_cast(bf16x2, 0x3f803f80u), acc, false);
}
DEV u32x4 neg4(u32x4 v) { return v ^ 0x80008000u; }
template <int CTRL> DEV float dpp_add(float v) {
  return v + __builtin_bit_cast(float, __builtin_amdgcn_update_dpp(0, __builtin_bit_cast(int, v), CTRL, 0xf, 0xf, true));
}
DEV float wave_sum(float v) {
  v = dpp_add<0xB1>(v);
  v = dpp_add<0x4E>(v);
  v = dpp_add<0x141>(v);
  v = dpp_add<0x140>(v);
  const int i = __builtin_bit_cast(int, v);
  return __builtin_bit_cast(float, __builtin_amdgcn_readlane(i, 0)) + __builtin_bit_cast(float, __builtin_amdgcn_readlane(i, 16)) +
         (__builtin_bit_cast(float, __builtin_amdgcn_readlane(i, 32)) + __builtin_bit_cast(float, __builtin_amdgcn_readlane(i, 48)));
}

struct RowDft {
  u32x4 c1[2], s1[2], a2;
  float twr[8], twi[8];
};
DEV void rowdft_init(RowDft& q, int l) {
  const int c = l & 15, g = l >> 4;
  const float fj = (float)(1 + c), fg = (float)(4 * g);
  const unsigned odd = (c & 1) ? 0u : 0x80008000u;
#pragma unroll
  for (int jj = 0; jj < 2; ++jj) {
    const float r0 = (fg + (float)(2 * jj)) * fj * (1.f / 32), r1 = r0 + fj * (1.f / 32);
    const unsigned cc = pk(cosr(r0), cosr(r1)), ss = pk(sinr(-r0), sinr(-r1));
    q.c1[0][jj] = cc; q.c1[0][jj + 2] = cc ^ odd;
    q.s1[0][jj] = ss; q.s1[0][jj + 2] = ss ^ odd;
  }
  q.c1[1] = q.c1[0] ^ 0x80000000u;
  q.s1[1] = q.s1[0] ^ 0x80000000u;
#pragma unroll
  for (int t = 0; t < 2; ++t) {
    const float dj = (fj + (float)(16 * t)) * (1.f / 512);
#pragma unroll
    for (int r = 0; r < 4; ++r) {
      const float rv = (fg + (float)r) * dj;
      q.twr[4 * t + r] = cosr(rv);
      q.twi[4 * t + r] = sinr(-rv);
    }
  }
  const float f2 = (float)(c >> 1) * (1.f / 16), sh = (c & 1) ? 0.25f : 0.f;
#pragma unroll
  for (int jj = 0; jj < 2; ++jj) {
    const float r0 = (fg + (float)(2 * jj)) * f2 + sh, r1 = r0 + f2;
    q.a2[jj] = pk(cosr(r0) * (1.f / 16), cosr(r1) * (1.f / 16));
    q.a2[jj + 2] = pk(sinr(r0) * (1.f / 16), sinr(r1) * (1.f / 16));
  }
}
DEV void rowdft(const RowDft& q, u32x4 a, int l, float (&o)[8]) {
  const f32x4 z = {0.f, 0.f, 0.f, 0.f};
#pragma unroll
  for (int t = 0; t < 2; ++t) {
    const f32x4 tr = mma16(a, q.c1[t], z), ti = mma16(a, q.s1[t], z);
    float ur[4], ui[4];
#pragma unroll
    for (int r = 0; r < 4; ++r) {
      ur[r] = tr[r] * q.twr[4 * t + r] - ti[r] * q.twi[4 * t + r];
      ui[r] = tr[r] * q.twi[4 * t + r] + ti[r] * q.twr[4 * t + r];
    }
    const u32x4 b = {pk(ur[0], ur[1]), pk(ur[2], ur[3]), pk(ui[0], ui[1]), pk(ui[2], ui[3])};
    const f32x4 x = mma16(q.a2, b, z);
#pragma unroll
    for (int r = 0; r < 4; ++r) o[4 * t + r] = x[r];
  }
  o[6] *= (l == 63) ? 0.70710678118654752f : 1.f;
  o[7] = (l == 63) ? 0.f : o[7];
}

DEV int d_of_m(int m) { return 16 * ((m >> 2) & 1) + (m & 3) + 4 * (m >> 3); }

typedef __amdgpu_buffer_rsrc_t rsrc_t;
DEV u32x4 ld4(rsrc_t r, unsigned v, unsigned s) { return __builtin_amdgcn_raw_buffer_load_b128(r, v, s, 16); }
DEV unsigned ld1(rsrc_t r, unsigned v, unsigned s) { return __builtin_amdgcn_raw_buffer_load_b32(r, v, s, 16); }
DEV void st4(u32x4 d, rsrc_t r, unsigned v, unsigned s) { __builtin_amdgcn_raw_buffer_store_b128(d, r, v, s, 16); }
DEV void st1(unsigned d, rsrc_t r, unsigned v, unsigned s) { __builtin_amdgcn_raw_buffer_store_b32(d, r, v, s, 16); }
DEV rsrc_t mk(const void* p, unsigned n) { return __builtin_amdgcn_make_buffer_rsrc((void*)p, 0, n, 0x00020000); }
DEV u32x4 in4(rsrc_t r, unsigned v, unsigned s) { return __builtin_amdgcn_raw_buffer_load_b128(r, v, s, 0); }
constexpr unsigned OOB = 0x40000000u;
DEV void drain() { asm volatile("s_waitcnt vmcnt(0)" ::: "memory"); }
DEV void bar() { asm volatile("s_waitcnt lgkmcnt(0)\n\ts_barrier" ::: "memory"); }
DEV void fence() { asm volatile("" ::: "memory"); __builtin_amdgcn_sched_barrier(0); }
template <int N> DEV void waitvm() { asm volatile("s_waitcnt vmcnt(%0)" :: "i"(N) : "memory"); }
DEV void keep(u32x4& v) { asm volatile("" : "+v"(v)); }
DEV void keep(float& v) { asm volatile("" : "+v"(v)); }
DEV bool any(bool p) { return __builtin_amdgcn_ballot_w64(p) != 0; }
DEV unsigned umin(unsigned a, unsigned b) { return a < b ? a : b; }
DEV unsigned umax(unsigned a, unsigned b) { return a > b ? a : b; }
DEV u32x4 clean(u32x4 v) {
  return u32x4{umin(v[0], 0xfffffffeu), umin(v[1], 0xfffffffeu), umin(v[2], 0xfffffffeu), umin(v[3], 0xfffffffeu)};
}
DEV void sum2(float a, float b, float& sa, float& sb) {
  const auto q = __builtin_amdgcn_permlane32_swap(__builtin_bit_cast(unsigned, a), __builtin_bit_cast(unsigned, b), false, false);
  const unsigned q0 = q[0], q1 = q[1];
  float v = __builtin_bit_cast(float, q0) + __builtin_bit_cast(float, q1);
  v = dpp_add<0xB1>(v);
  v = dpp_add<0x4E>(v);
  v = dpp_add<0x141>(v);
  v = dpp_add<0x140>(v);
  const int u = __builtin_bit_cast(int, v);
  sa = __builtin_bit_cast(float, __builtin_amdgcn_readlane(u, 0)) + __builtin_bit_cast(float, __builtin_amdgcn_readlane(u, 16));
  sb = __builtin_bit_cast(float, __builtin_amdgcn_readlane(u, 32)) + __builtin_bit_cast(float, __builtin_amdgcn_readlane(u, 48));
}
DEV float xsum(float v) {
  const unsigned u = __builtin_bit_cast(unsigned, v);
  const auto a = __builtin_amdgcn_permlane16_swap(u, u, false, false);
  const unsigned a0 = a[0], a1 = a[1];
  const float s = __builtin_bit_cast(float, a0) + __builtin_bit_cast(float, a1);
  const unsigned su = __builtin_bit_cast(unsigned, s);
  const auto q = __builtin_amdgcn_permlane32_swap(su, su, false, false);
  const unsigned q0 = q[0], q1 = q[1];
  return __builtin_bit_cast(float, q0) + __builtin_bit_cast(float, q1);
}

__global__ __launch_bounds__(256, 1) void fused(const u16* __restrict__ x, const u16* __restrict__ wr, const u16* __restrict__ wi,
                                                const u16* __restrict__ w1, const u16* __restrict__ b1, const u16* __restrict__ w2,
                                                const u16* __restrict__ b2, char* ws, u16* __restrict__ out) {
  __shared__ __attribute__((aligned(16))) char smem[L_END];
  LAS char* sm = (LAS char*)smem;
  const int b = blockIdx.x, tid = threadIdx.x, l = tid & 63;
  const int w = __builtin_amdgcn_readfirstlane(tid >> 6);
  const int xcd = b & 7, i = b >> 3, pos = xcd * 32 + i;
  const int c = l & 15, g = l >> 4;
  const rsrc_t rw = __builtin_amdgcn_make_buffer_rsrc(ws, 0, OFF_END, 0x00020000);
  const f32x4 z4 = {0.f, 0.f, 0.f, 0.f};
  unsigned tp0;
  {
    const char* pc;
    asm volatile("s_getpc_b64 %0" : "=s"(pc));
    tp0 = __builtin_amdgcn_raw_buffer_load_b32(mk(pc, CODE), tid * 128, 0, 0);
  }
  fence();
  asm volatile("" :: "s"(x), "s"(wr), "s"(wi), "s"(w1), "s"(b1), "s"(w2), "s"(b2), "s"(ws), "s"(out));
  STAMP_DECL
  STAMP(0);
  const int s0 = 32 * (32 * (xcd >> 2) + 16 * (w >> 1) + 4 * (xcd & 3) + 2 * (w & 1)) + 8 * ((i & 15) >> 2) + 4 * (i >> 4) + (i & 3);
  const rsrc_t rx = mk(x, S_ * D_ * 2);
  const u32x4 xa = in4(rx, l * 16, s0 * (D_ * 2)), xb = in4(rx, l * 16, (s0 + 32) * (D_ * 2));
  const unsigned ep = ld1(rw, 0, OFF_E);
  fence();
  const int kp = 8 * ((pos & 15) + 16 * (pos >> 6)) + 4 * ((pos >> 4) & 1) + 2 * ((pos >> 5) & 1);
  const int jc = 16 * w + c;
  const unsigned vh = (jc >> 1) * 65536 + (jc & 1) * 64 + g * 16;
  st4(u32x4{~0u, ~0u, ~0u, ~0u}, rw, vh, OFF_H + kp * 128);
  st4(u32x4{~0u, ~0u, ~0u, ~0u}, rw, vh + 128, OFF_H + kp * 128);
  fence();
  const int j0 = 16 * (pos >> 6) + (pos & 15) + 512 * ((pos >> 4) & 3);
  u16 fr[8], fi[8];
#pragma unroll
  for (int e = 0; e < 8; ++e) fr[e] = __builtin_amdgcn_raw_buffer_load_b16(mk(wr, S_ * DF_ * 2), 2 + 2 * tid, (j0 + 64 * e) * (DF_ * 2), 0);
  if (FPOS == 0) {
#pragma unroll
      for (int e = 0; e < 8; ++e) fi[e] = __builtin_amdgcn_raw_buffer_load_b16(mk(wi, S_ * DF_ * 2), 2 + 2 * tid, (j0 + 64 * e) * (DF_ * 2), 0);
  }
  fence();
  const int j1 = xcd * 22 + i;
  const bool has1 = i < 22 && j1 < F_ / 8;
  const int j2 = 3 * pos + w;
  const bool has2 = w < 3 && j2 < KS2 * 8;
  u32x4 v0, v1, y0, y1;
  unsigned tc0, tc1;
  const rsrc_t r1 = mk(w1, D_ * F_ * 2);
  {
    tc0 = __builtin_amdgcn_raw_buffer_load_b32(r1, l < 16 ? (16 * i + 4 * w + (l >> 2)) * (F_ * 2) + xcd * 352 + umin((l & 3) * 128, 348) : OOB, 0, 0);
  }
  {
    const rsrc_t r2 = mk(w2, D_ * F_ * 2);
    const unsigned o2 = has2 ? (16 * (j2 >> 3) + (l >> 3)) * (D_ * 2) + (j2 & 7) * 128 + (l & 7) * 16 : OOB;
    y0 = in4(r2, o2, 0);
    y1 = in4(r2, o2, 8 * D_ * 2);
  }
  fence();
  const int grp = XP ? 8 * (xcd >> 1) + (i >> 2) : 4 * xcd + (i >> 3), cu = XP ? 4 * (xcd & 1) + (i & 3) : i & 7;
  const int T0 = (cu < 6 ? 11 * cu : 10 * cu + 6) + ((cu >= 6 && w == 3) ? 8 : 3 * w);
  const bool three = w < 2 || (w == 2 && cu < 6);
  const int rot = cu & 3;
  fence();
  STAMP(20);
  RowDft q;
  rowdft_init(q, l);
  keep(q.c1[0]); keep(q.c1[1]); keep(q.s1[0]); keep(q.s1[1]); keep(q.a2);
#pragma unroll
  for (int e = 0; e < 8; ++e) { keep(q.twr[e]); keep(q.twi[e]); }
  fence();
  if (FPOS == 1) {
#pragma unroll
      for (int e = 0; e < 8; ++e) fi[e] = __builtin_amdgcn_raw_buffer_load_b16(mk(wi, S_ * DF_ * 2), 2 + 2 * tid, (j0 + 64 * e) * (DF_ * 2), 0);
  }
  fence();
  STAMP(21);
  const unsigned vgat = has1 ? tid * (F_ * 2) + j1 * 16 : OOB;
#define GLD(so) __builtin_amdgcn_raw_buffer_load_b128(r1, vgat, so, GAUX)
#define GMARK(t, sp) st4(u32x4{~0u, ~0u, ~0u, ~0u}, rw, (((t) < 2 || three) ? l * 16 : OOB) + (sp) * 1024, OFF_G + (grp * KS2 + T0 + (t)) * 2048);
  const int kw = w >> 1;
  u32x4 c1[2], s1[2], c2[2], s2[2], sel;
  float t1r[8], t1i[8], t2r[8], t2i[8];
  auto consts = [&]() {
  float fjc = (float)jc, fg = (float)g;
  keep(fjc); keep(fg);
  {
    const float dj = fjc * (1.f / 64), n0 = 4.f * fg + (float)(32 * kw);
#pragma unroll
    for (int jj = 0; jj < 4; ++jj) {
      const float r0 = (n0 + (float)(16 * (jj >> 1) + ((2 * jj) & 3))) * dj, r1 = r0 + dj;
      c1[0][jj] = pk(cosr(r0), cosr(r1));
      s1[0][jj] = pk(sinr(r0), sinr(r1));
    }
    const unsigned odd = (c & 1) ? 0x80008000u : 0u;
    c1[1] = c1[0] ^ odd;
    s1[1] = s1[0] ^ odd;
  }
  keep(c1[0]); keep(c1[1]); keep(s1[0]); keep(s1[1]);
#pragma unroll
  for (int t = 0; t < 2; ++t) {
    if (GPOS == 2) { fence(); GMARK(0, t) fence(); keep(fg); }
    const float sg = (float)(8 * (c >> 2) + 4 * t + (c & 3));
#pragma unroll
    for (int jj = 0; jj < 4; ++jj) {
      const float r0 = (8.f * fg + (float)(2 * jj)) * (sg * (1.f / 32)), r1 = r0 + sg * (1.f / 32);
      c2[t][jj] = pk(cosr(r0), cosr(r1));
      s2[t][jj] = pk(sinr(r0), sinr(r1));
    }
    if (GPOS == 2) { keep(c2[t]); keep(s2[t]); fence(); GMARK(1, t) fence(); keep(fg); }
#pragma unroll
    for (int r = 0; r < 4; ++r) {
      const float a = (8.f * fg + (float)(4 * t + r)) * (fjc * (1.f / 2048));
      t1r[4 * t + r] = cosr(a);
      t1i[4 * t + r] = sinr(-a);
      const float bq = (4.f * fg + (float)(16 * w + r)) * (sg * (1.f / 2048));
      t2r[4 * t + r] = cosr(bq) * (1.f / 2048);
      t2i[4 * t + r] = sinr(bq) * (1.f / 2048);
    }
    if (GPOS == 2) { keep(t1r[4 * t + 3]); keep(t2i[4 * t + 3]); fence(); GMARK(2, t) fence(); }
  }
#pragma unroll
  for (int jj = 0; jj < 4; ++jj) {
    const bool on = ((jj >> 1) == (w & 1)) && ((c >> 2) == g) && (((c & 3) >> 1) == (jj & 1));
    sel[jj] = on ? ((c & 1) ? 0x3f800000u : 0x00003f80u) : 0u;
  }
  keep(c2[0]); keep(c2[1]); keep(s2[0]); keep(s2[1]); keep(sel);
#pragma unroll
  for (int e = 0; e < 8; ++e) { keep(t1r[e]); keep(t1i[e]); keep(t2r[e]); keep(t2i[e]); }
  };
  if (CPOS == 0) consts();
  fence();
  fence();
  STAMP(22);
  const int n = l & 31, h = l >> 5, wk = (w + rot) & 3;
  u32x4 wa[3][16];
  const unsigned vw3 = three ? l * 16 : OOB;
#define LDW1(ks)                                                                                         \
  { const unsigned so_ = OFF_W1F + T0 * 16384 + ((((ks) >> 2) + rot) & 3) * 4096;                        \
    wa[0][ks] = ld4(rw, l * 16 + ((ks) & 3) * 1024, so_);                                                \
    wa[1][ks] = ld4(rw, l * 16 + ((ks) & 3) * 1024, so_ + 16384);                                        \
    wa[2][ks] = ld4(rw, vw3 + ((ks) & 3) * 1024, so_ + 32768); }
#define LDW1T(t, ks) wa[t][ks] = ld4(rw, ((t) < 2 ? l * 16 : vw3) + ((ks) & 3) * 1024, OFF_W1F + (T0 + (t)) * 16384 + ((((ks) >> 2) + rot) & 3) * 4096);
  const unsigned va = (2 * T0 + (l < (three ? 6 : 4) ? l : 0)) * 128;
  unsigned auxv, tag;
  auto auxchk = [&]() {
  if (any(auxv != tag)) {
#pragma unroll 1
    for (int spin = 0; spin < SPIN; ++spin) {
      asm volatile("" ::: "memory");
      if (!any(ld1(rw, va, OFF_AUX + xcd * 176 * 128) != tag)) break;
    }
  }
  };
  auto basis = [&]() {
  int nz = n;
  asm volatile("" : "+v"(nz));
#pragma unroll
  for (int dd = 0; dd < 2; ++dd) {
    if (GAT == 0) { fence(); if (dd) v1 = GLD(256 * F_ * 2); else v0 = GLD(0); fence(); }
    const int d = 64 * cu + 32 * (dd ^ (w & 1)) + d_of_m(nz);
    const float dl = (float)d * (1.f / 512);
    const float cd = cosr(dl), sn = sinr(dl);
    const f32x2 rc = {cd, cd}, rs = {sn, -sn};
    f32x2 p[2];
#pragma unroll
    for (int r = 0; r < 2; ++r) {
      const float a = (float)(((1 + 16 * h + 32 * r + 64 * wk) * d) & 511) * (1.f / 512);
      p[r] = f32x2{cosr(a) * 0.0625f, sinr(a) * -0.0625f};
    }
#pragma unroll
    for (int ii = 0; ii < 8; ++ii) {
      if (GPOS == 0 && (ii & 1) && ii < 6) { fence(); GMARK(ii >> 1, dd) fence(); }
      if (BASPOS == 2 && (8 * dd + ii + 1) * NB2 / 16 != (8 * dd + ii) * NB2 / 16) { fence(); LDW1((8 * dd + ii) * NB2 / 16); fence(); }
      if (BASPOS == 3 && dd == 1) {
        fence();
        if (ii == 0) auxchk();
#pragma unroll
        for (int ks = ii * NB2 / 8; ks < (ii + 1) * NB2 / 8; ++ks) LDW1(ks);
        fence();
      }
      u32x4 f;
#pragma unroll
      for (int r = 0; r < 2; ++r) {
        f[2 * r] = pk(p[r][0], p[r][1]);
        p[r] = p[r] * rc + f32x2{p[r][1], p[r][0]} * rs;
        if (ii == 7 && r == 1) p[r][0] *= (wk == 3 && h == 1) ? 0.70710678118654752f : 1.f;
        f[2 * r + 1] = pk(p[r][0], p[r][1]);
        p[r] = p[r] * rc + f32x2{p[r][1], p[r][0]} * rs;
      }
      *(LAS u32x4*)(sm + L_BAS + w * 16384 + (2 * ii + dd) * 1024 + l * 16) = f;
    }
  }
  };
  auto job2 = [&]() {
    LAS char* mine = sm + L_H + 49152 + w * 2048;
    *(LAS u32x4*)(mine + l * 16) = y0;
    *(LAS u32x4*)(mine + 1024 + l * 16) = y1;
    LAS char* rd = mine + (8 * (g >> 1) + (c >> 2)) * 128 + (16 * (c & 1) + 8 * (g & 1) + 4 * ((c >> 1) & 1)) * 2;
#pragma unroll
    for (int dt = 0; dt < 2; ++dt) {
      const u32x2 a0 = tr_read(rd + dt * 64), a1 = tr_read(rd + dt * 64 + 512);
      st4(u32x4{a0[0], a0[1], a1[0], a1[1]}, rw, has2 ? l * 16 + ((2 * (j2 & 7) + dt) * KS2 + (j2 >> 3)) * 1024 : OOB, OFF_W2F);
    }
  };
  if (BASPOS == 0) basis();
  fence();
  tag = __builtin_amdgcn_readfirstlane(ep) + 1u;
  {
    LAS char* mine = sm + L_TMP + w * 2048;
    *(LAS u32x4*)(mine + l * 16) = xa;
    *(LAS u32x4*)(mine + 1024 + l * 16) = xb;
    STAMP(1);
    unsigned fw[8];
#pragma unroll
    for (int e = 0; e < 8; ++e) fw[e] = (unsigned)fr[e] | ((unsigned)fi[e] << 16);
    *(LAS u32x4*)(sm + L_H + 16384 + tid * 48) = u32x4{fw[0], fw[1], fw[2], tag};
    *(LAS u32x4*)(sm + L_H + 16384 + tid * 48 + 16) = u32x4{fw[3], fw[4], fw[5], tag};
    *(LAS u32x4*)(sm + L_H + 16384 + tid * 48 + 32) = u32x4{fw[6], fw[7], 0u, tag};
#pragma unroll
    for (int it = 0; it < 3; ++it)
      st4(*(LAS u32x4*)(sm + L_H + 16384 + w * 3072 + l * 16 + it * 1024), rw, w * 3072 + l * 16 + it * 1024, OFF_W + pos * ZSTR);
    if (JOBPOS == 5) job2();
    if (GAT == 2) { v0 = GLD(0); v1 = GLD(256 * F_ * 2); }
    fence();
    float o[2][8];
#pragma unroll
    for (int rr = 0; rr < 2; ++rr) {
      const u32x2 a0 = tr_read(mine + rr * 1024 + l * 8), a1 = tr_read(mine + rr * 1024 + 512 + l * 8);
      const u32x4 a = {a0[0], a0[1], a1[0], a1[1]};
      rowdft(q, a, l, o[rr]);
    }
    float rs[2], sdv[2];
    {
      float e0 = 0.f, e1 = 0.f;
#pragma unroll
      for (int e = 0; e < 8; ++e) { e0 += o[0][e] * o[0][e]; e1 += o[1][e] * o[1][e]; }
      float t0, t1;
      sum2(e0, e1, t0, t1);
      const float va = t0 * (1.f / D_) + EPS, vb = t1 * (1.f / D_) + EPS;
      rs[0] = __builtin_amdgcn_rsqf(va); rs[1] = __builtin_amdgcn_rsqf(vb);
      sdv[0] = va * rs[0]; sdv[1] = vb * rs[1];
    }
    const unsigned sp = pk(sdv[0], sdv[1]);
#pragma unroll
    for (int e = 0; e < 4; ++e) {
      const int kk = c + 16 * (e >> 1) + 32 * (e & 1) + 64 * g;
      *(LAS u32x4*)(sm + L_H + kk * 64 + ((w ^ ((c >> 1) & 3)) * 16)) =
          u32x4{pk(o[0][2 * e] * rs[0], o[1][2 * e] * rs[1]), pk(o[0][2 * e + 1] * rs[0], o[1][2 * e + 1] * rs[1]), sp, tag};
    }
    waitvm<22 + (BASPOS == 0 && GPOS == 0 ? 6 : 0) + (GAT != 1 ? 2 : 0) + (JOBPOS == 5 ? 2 : 0)>();
    bar();
    STAMP(2);
#pragma unroll
    for (int it = 0; it < 4; ++it) {
      const int p = tid + 256 * it;
      st4(*(LAS u32x4*)(sm + L_H + (p >> 2) * 64 + (((p & 3) ^ ((p >> 3) & 3)) * 16)), rw, tid * 16 + it * 4096, OFF_Z + pos * ZSTR);
    }
    fence();
    STAMP(3);
  }
  fence();
  asm volatile("" :: "v"(tc0), "v"(tp0));
  const unsigned vt = XP ? (l < 43 ? (((xcd & 1) * 128 + i * 4 + w) * 43 + l) * 128 : OOB) : (l < 43 ? ((i * 4 + w) * 86 + l) * 128 : OOB);
  const unsigned vu = XP ? OOB : vt;
  const unsigned vt1 = XP ? (l < 44 - 2 * (xcd & 1) ? ((xcd & 1) * 128 * 44 + (i * 4 + w) * (44 - 2 * (xcd & 1)) + l) * 128 : OOB) : vt;
  unsigned tc2 = 0, tc3 = 0;
  if (TW2 >= 5) tc2 = ld1(rw, vt, OFF_W2F);
  if (TW2 == 5) tc3 = ld1(rw, vu, OFF_W2F + 43 * 128);
  fence();
  if (CPOS == 2) consts();
  fence();
  if (GAT == 1) { v0 = GLD(0); v1 = GLD(256 * F_ * 2); }
  fence();
  STAMP(4);
  fence();
  {
    u32x4 zp[4], wp[3];
    const unsigned vz = (tid >> 2) * ZSTR + (tid & 3) * 16;
    unsigned vw[3];
#pragma unroll
    for (int it = 0; it < 3; ++it) { const unsigned p = tid + 256 * it, pp = p / 3u; vw[it] = pp * ZSTR + (p - 3 * pp) * 16; }
#define POLLZ _Pragma("unroll") for (int it = 0; it < 4; ++it) zp[it] = ld4(rw, vz, OFF_Z + pos * 64 + it * 64 * ZSTR);
#define POLLW _Pragma("unroll") for (int it = 0; it < 3; ++it) wp[it] = ld4(rw, vw[it], OFF_W + pos * 48);
    auto job1 = [&]() {
#pragma unroll
  for (int it = 0; it < 2; ++it) {
    const int ll = (tid >> 3) + 32 * it;
    st4(*(LAS u32x4*)(sm + L_H + 32768 + 8192 + ll * 128 + (((tid & 7) ^ (ll & 7)) * 16)), rw,
        has1 ? ll * 256 + (tid & 7) * 16 + (j1 >> 1) * 16384 + (j1 & 1) * 128 : OOB, OFF_W1F);
  }
    };
    auto jobs = [&]() {
    if (JOBPOS < 3) job1();
    if (JOBPOS != 5) job2();
    float m0 = 0.f, m1 = 0.f;
#pragma unroll
    for (int e = 0; e < 4; ++e) { m0 = sum2b(xa[e], m0); m1 = sum2b(xb[e], m1); }
    sum2(m0, m1, m0, m1);
    st1(__builtin_bit_cast(unsigned, (l ? m1 : m0) * (1.f / D_)), rw, l < (ML ? 32 : 2) ? l * 4 : OOB, OFF_MEAN + (pos * 4 + w) * 128);
    };
    fence();
    POLLW
    fence();
    if (BASPOS == 1) basis();
    fence();
    STAMP(23);
    if (JOBPOS >= 4) jobs();
    fence();
    if (PZ == 1) { POLLZ }
    fence();
  if (has1) {
    *(LAS u32x4*)(sm + L_H + 32768 + tid * 16) = v0;
    *(LAS u32x4*)(sm + L_H + 32768 + 4096 + tid * 16) = v1;
    bar();
    if (PZ == 2) { POLLZ fence(); }
    unsigned dw[8];
#pragma unroll
    for (int j = 0; j < 8; ++j) dw[j] = *(LAS unsigned*)(sm + L_H + 32768 + (16 * (4 * g + (j & 3) + 16 * (j >> 2)) + c) * 16 + w * 4);
#pragma unroll
    for (int rr = 0; rr < 2; ++rr) {
      u32x4 a;
#pragma unroll
      for (int jj = 0; jj < 4; ++jj) a[jj] = __builtin_amdgcn_perm(dw[2 * jj + 1], dw[2 * jj], rr ? 0x07060302u : 0x05040100u);
      float o[8];
      if (PZ == 3 && rr == 1) { fence(); POLLZ fence(); }
      rowdft(q, a, l, o);
      *(LAS u32x4*)(sm + L_H + 32768 + 8192 + l * 128 + (((2 * w + rr) ^ (l & 7)) * 16)) =
          u32x4{pk(o[0], o[1]), pk(o[2], o[3]), pk(o[4], o[5]), pk(o[6], o[7])};
    }
    bar();
  } else if (PZ >= 2) { POLLZ }
    if (JOBPOS == 0) jobs();
    if (JOBPOS >= 3 && JOBPOS < 6) job1();
    fence();
    STAMP(5);
    if (TW2 == 3) { tc2 = ld1(rw, vt, OFF_W2F); tc3 = ld1(rw, vu, OFF_W2F + 43 * 128); fence(); }
    if (PZ == 0) { POLLZ }
    fence();
    if (CPOS == 1) consts();
    fence();
    if (JOBPOS == 1 || JOBPOS == 3) jobs();
    fence();
#define BADZ any((((zp[0][3] ^ tag) | (zp[1][3] ^ tag)) | ((zp[2][3] ^ tag) | (zp[3][3] ^ tag))) != 0)
#define BADW any((((wp[0][3] ^ tag) | (wp[1][3] ^ tag)) | (wp[2][3] ^ tag)) != 0)
    if (BADW) {
#pragma unroll 1
      for (int spin = 0; spin < SPIN; ++spin) {
        asm volatile("" ::: "memory");
        POLLW
        if (!BADW) break;
      }
    }
#pragma unroll
    for (int it = 0; it < 3; ++it) *(LAS u32x4*)(sm + L_TMP + 20480 + it * 4096 + tid * 16) = wp[it];
    fence();
    if (BADZ) {
#pragma unroll 1
      for (int spin = 0; spin < SPIN; ++spin) {
        asm volatile("" ::: "memory");
        POLLZ
        if (!BADZ) break;
      }
    }
    if (JOBPOS == 3) waitvm<3 + (GPOS == 2 ? 6 : 0)>(); else if (DRPOS == 0) drain();
    STAMP(6);
    if (JOBPOS == 6) { job1(); fence(); }
#pragma unroll
    for (int it = 0; it < 4; ++it) {
      const int pp = 64 * it + (tid >> 2), qq = tid & 3, px = pp >> 5, pi = pp & 31;
      *(LAS unsigned*)(sm + L_TMP + 8192 + it * 1024 + tid * 4) = zp[it][0];
      *(LAS unsigned*)(sm + L_TMP + 12288 + it * 1024 + tid * 4) = zp[it][1];
      const int s = 32 * (32 * (px >> 2) + 16 * (qq >> 1) + 4 * (px & 3) + 2 * (qq & 1)) + 8 * ((pi & 15) >> 2) + 4 * (pi >> 4) + (pi & 3);
      *(LAS u16*)(sm + L_TMP + 16384 + s * 2) = (u16)zp[it][2];
      *(LAS u16*)(sm + L_TMP + 16384 + s * 2 + 64) = (u16)(zp[it][2] >> 16);
    }
    bar();
    if (DRPOS == 0) st1(tag, rw, (has1 && tid < 8) ? OFF_AUX + (tid * 176 + j1) * 128 : ((b == 0 && tid == 8) ? OFF_E : OOB), 0);
    if (JOBPOS == 2) jobs();
    STAMP(18);
  }
  fence();
  if (GPOS == 1) { GMARK(0, 0) GMARK(0, 1) GMARK(1, 0) GMARK(1, 1) GMARK(2, 0) GMARK(2, 1) fence(); }
  if (TW2 == 4) { tc2 = ld1(rw, vt, OFF_W2F); tc3 = ld1(rw, vu, OFF_W2F + 43 * 128); }
  if (DRPOS == 0) { tc0 = ld1(rw, vt1, OFF_W1F); tc1 = ld1(rw, vu, OFF_W1F + 43 * 128); }
  unsigned tq = 0, tm = 0;
#define LDTM tm = ld1(rw, 0, OFF_MEAN + (pos * 4 + w) * 128);
  if (OT) tq = __builtin_amdgcn_raw_buffer_load_b32(mk(out, S_ * D_ * 2), l < 16 ? ((64 * grp + 16 * w + l) * D_ + 64 * cu) * 2 : OOB, 0, OT == 1 ? 16 : 0);
  unsigned ta = 0, td = 0;
#define LDTA { if (TB & 1) ta = __builtin_amdgcn_raw_buffer_load_b32(mk(b1, F_ * 2), 32 * T0 + (l & 1) * 64, 0, 0); \
               if (TB & 2) td = __builtin_amdgcn_raw_buffer_load_b32(mk(b2, D_ * 2), 128 * cu, 0, 0); }
  if (!(TB & 4)) LDTA
  fence();
  STAMP(7);
  {
    u32x4 zr[2][2], zi[2][2];
#pragma unroll
    for (int ks = 0; ks < 2; ++ks)
#pragma unroll
      for (int t = 0; t < 2; ++t) {
        LAS char* src = sm + L_TMP + 8192 + ((((ks ^ kw) * 4 + g) * 32) + 16 * t + c) * 16;
        zr[t][ks] = *(LAS u32x4*)src;
        zi[t][ks] = *(LAS u32x4*)(src + 4096);
      }
    const u32x4 sd = *(LAS u32x4*)(sm + L_TMP + 16384 + (32 * jc + 8 * g) * 2);
    const u32x4 wq0 = *(LAS u32x4*)(sm + L_TMP + 20480 + tid * 48), wq1 = *(LAS u32x4*)(sm + L_TMP + 20480 + tid * 48 + 16),
                wq2 = *(LAS u32x4*)(sm + L_TMP + 20480 + tid * 48 + 32);
    const u32x4 wf0 = {wq0[0], wq0[1], wq0[2], wq1[0]}, wf1 = {wq1[1], wq1[2], wq2[0], wq2[1]};
    u32x4 bre, bim;
    f32x4 hr[2], hi[2];
#pragma unroll
    for (int t = 0; t < 2; ++t) {
      f32x4 tr = mma16(zr[t][0], c1[0], z4);
      f32x4 ti = mma16(zi[t][0], c1[0], z4);
      tr = mma16(zi[t][0], s1[0], tr);
      ti = mma16(zr[t][0], neg4(s1[0]), ti);
      tr = mma16(zr[t][1], c1[1], tr);
      ti = mma16(zi[t][1], c1[1], ti);
      tr = mma16(zi[t][1], s1[1], tr);
      ti = mma16(zr[t][1], neg4(s1[1]), ti);
      float ur[4], ui[4];
#pragma unroll
      for (int r = 0; r < 4; ++r) {
        ur[r] = tr[r] * t1r[4 * t + r] - ti[r] * t1i[4 * t + r];
        ui[r] = tr[r] * t1i[4 * t + r] + ti[r] * t1r[4 * t + r];
      }
      bre[2 * t] = pk(ur[0], ur[1]); bre[2 * t + 1] = pk(ur[2], ur[3]);
      bim[2 * t] = pk(ui[0], ui[1]); bim[2 * t + 1] = pk(ui[2], ui[3]);
      const unsigned sa = sd[2 * t], sb = sd[2 * t + 1];
      const f32x4 sv = {lo16(sa), hi16(sa), lo16(sb), hi16(sb)};
      hr[t] = mma16(zr[t][0], sel, z4) * sv;
      hi[t] = mma16(zi[t][0], sel, z4) * sv;
    }
    u32x4 are, aim;
#pragma unroll
    for (int t = 0; t < 2; ++t) {
      f32x4 xr = mma16(c2[t], bre, z4);
      f32x4 xi = mma16(c2[t], bim, z4);
      xr = mma16(s2[t], bim, xr);
      xi = mma16(neg4(s2[t]), bre, xi);
      const u32x4 wf = t ? wf1 : wf0;
      float ur[4], ui[4];
#pragma unroll
      for (int r = 0; r < 4; ++r) {
        const float a = lo16(wf[r]), bq = hi16(wf[r]);
        ur[r] = xr[r] * a - xi[r] * bq;
        ui[r] = xr[r] * bq + xi[r] * a;
      }
      are[2 * t] = pk(ur[0], ur[1]); are[2 * t + 1] = pk(ur[2], ur[3]);
      aim[2 * t] = pk(ui[0], ui[1]); aim[2 * t + 1] = pk(ui[2], ui[3]);
    }
#pragma unroll
    for (int t = 0; t < 2; ++t) {
      f32x4 vr = mma16(are, c2[t], z4);
      f32x4 vi = mma16(aim, c2[t], z4);
      vr = mma16(aim, neg4(s2[t]), vr);
      vi = mma16(are, s2[t], vi);
      float ur[4], ui[4];
#pragma unroll
      for (int r = 0; r < 4; ++r) {
        ur[r] = vr[r] * t2r[4 * t + r] - vi[r] * t2i[4 * t + r];
        ui[r] = vr[r] * t2i[4 * t + r] + vi[r] * t2r[4 * t + r];
      }
      *(LAS u32x4*)(sm + L_TMP + (w * 2 + t) * 1024 + l * 16) = u32x4{pk(ur[0], ur[1]), pk(ur[2], ur[3]), pk(ui[0], ui[1]), pk(ui[2], ui[3])};
    }
    if (DRPOS == 1) waitvm<(TW2 == 4 ? 2 : 0) + (GF ? 6 : 0) + (OT ? 1 : 0) + ((TB & 4) ? 0 : (TB & 1) + (TB >> 1))>(); else if (GPOS == 1) waitvm<2 + (OT ? 1 : 0)>(); else if (JOBPOS == 3) waitvm<3 + (OT ? 1 : 0)>();
    bar();
    if (DRPOS == 0) auxv = ld1(rw, va, OFF_AUX + xcd * 176 * 128);
    if (DRPOS == 1) {
      if (FL) st4(u32x4{tag, tag, tag, tag}, rw, (has1 && tid < 64) ? OFF_AUX + ((tid >> 3) * 176 + j1) * 128 + (tid & 7) * 16 : ((b == 0 && tid == 64) ? OFF_E : OOB), 0);
      else st1(tag, rw, (has1 && tid < 8) ? OFF_AUX + (tid * 176 + j1) * 128 : ((b == 0 && tid == 8) ? OFF_E : OOB), 0);
      tc0 = ld1(rw, vt1, OFF_W1F); tc1 = ld1(rw, vu, OFF_W1F + 43 * 128);
      if (TM == 2) { fence(); LDTM }
      if (TB & 4) { fence(); LDTA }
    }
    fence();
    STAMP(19);
#pragma unroll
    for (int ks = 0; ks < 2; ++ks)
#pragma unroll
      for (int hh = 0; hh < 2; ++hh) {
        const int src = 2 * (ks ^ kw) + hh;
        const u32x4 br = {c1[ks][2 * hh], c1[ks][2 * hh + 1], s1[ks][2 * hh] ^ 0x80008000u, s1[ks][2 * hh + 1] ^ 0x80008000u};
        const u32x4 bi = {s1[ks][2 * hh], s1[ks][2 * hh + 1], c1[ks][2 * hh], c1[ks][2 * hh + 1]};
#pragma unroll
        for (int t = 0; t < 2; ++t) {
          const u32x4 u = *(LAS u32x4*)(sm + L_TMP + (src * 2 + t) * 1024 + l * 16);
          hr[t] = mma16(u, br, hr[t]);
          hi[t] = mma16(u, bi, hi[t]);
        }
      }
    const u32x4 ore = {pk(hr[0][0], hr[0][1]), pk(hr[0][2], hr[0][3]), pk(hr[1][0], hr[1][1]), pk(hr[1][2], hr[1][3])};
    u32x4 oim = {pk(hi[0][0], hi[0][1]), pk(hi[0][2], hi[0][3]), pk(hi[1][0], hi[1][1]), pk(hi[1][2], hi[1][3])};
    if (pos == 255) oim = u32x4{0u, 0u, 0u, 0u};
    st4(clean(ore), rw, vh, OFF_H + kp * 128);
    st4(clean(oim), rw, vh + 128, OFF_H + kp * 128);
  }
  fence();
  if (DRPOS == 1) auxv = ld1(rw, va, OFF_AUX + xcd * 176 * 128);
  u32x2 e1[3];
#define LDE1 _Pragma("unroll") for (int t = 0; t < 3; ++t) e1[t] = __builtin_amdgcn_raw_buffer_load_b64(mk(b1, F_ * 2), (16 * (T0 + t) + 4 * g) * 2, 0, 0);
  if (EF == 1) { fence(); LDE1 fence(); }
  unsigned tb = 0, mu = 0;
#define LDTB tb = __builtin_amdgcn_raw_buffer_load_b32(mk(b2, D_ * 2), 128 * cu, 0, 0);
  if (EB == 1) { LDTB fence(); }
  if (TM == 1) { LDTM fence(); }
  const int m1 = 2 * grp + (w >> 1);
#define LDMU mu = ld1(rw, (16 * ((n >> 2) & 1) + 4 * (n >> 3) + (n & 3)) * 512, \
                      OFF_MEAN + ((4 * (m1 >> 5) + ((m1 >> 2) & 3)) * 128 + 2 * ((m1 >> 4) & 1) + ((m1 >> 1) & 1)) * 128 + (m1 & 1) * 4);
  if (TW2 == 1 || TW2 == 2) tc2 = ld1(rw, vt, OFF_W2F);
  if (TW2 == 2) tc3 = ld1(rw, vu, OFF_W2F + 43 * 128);
  fence();
  STAMP(8);
  if (BASPOS != 3) auxchk();
  fence();
  if (BASPOS >= 2) basis();
  else {
#pragma unroll
    for (int ks = 0; ks < NB2; ++ks) LDW1(ks);
  }
  fence();
  if (EF == 2) { LDE1 fence(); }
  STAMP(9);
  {
    const int row = tid >> 3, f = ((row >> 1) & 1) | (((row >> 3) & 1) << 1);
    LAS char* dst = sm + L_H + row * 128 + ((((tid & 7) >> 1) ^ f) * 32) + (tid & 1) * 16;
#pragma unroll 1
    for (int spin = 0; spin < SPIN; ++spin) {
      u32x4 hp[16];
      asm volatile("" ::: "memory");
#pragma unroll
      for (int it = 0; it < 16; ++it) hp[it] = ld4(rw, tid * 16, OFF_H + grp * 65536 + ((it + 4 * rot) & 15) * 4096);
      fence();
      unsigned m = 0;
#pragma unroll
      for (int it = 0; it < 16; ++it) {
        *(LAS u32x4*)(dst + it * 4096) = hp[it];
        m = umax(umax(m, umax(hp[it][0], hp[it][1])), umax(hp[it][2], hp[it][3]));
        fence();
      }
      if (!any(m == ~0u)) break;
    }
    STAMP(10);
    asm volatile("" :: "v"(tc0), "v"(tc1), "v"(tc2), "v"(tc3), "v"(tq), "v"(tb), "v"(tm), "v"(ta), "v"(td));
    if (TW2 < 1) tc0 = ld1(rw, vt, OFF_W2F);
    if (TW2 < 2 || TW2 == 6) tc1 = ld1(rw, vu, OFF_W2F + 43 * 128);
    if (GF) waitvm<2>();
    bar();
    if (GF) st1(tag, rw, tid < 8 ? (tid * 8 + cu) * 128 : OOB, OFF_F3 + grp * 8192);
    if (EM == 1) { LDMU }
    STAMP(16);
  }
  fence();
  const int k0 = 22 * w;
  u32x4 fv[2][22];
  unsigned gfl = 0;
  if (EF == 0) { LDE1 }
  const rsrc_t rb2 = mk(b2, D_ * 2);
  if (EM == 2) {
    mu = (16 * ((n >> 2) & 1) + 4 * (n >> 3) + (n & 3)) * 512 + OFF_MEAN + ((4 * (m1 >> 5) + ((m1 >> 2) & 3)) * 128 + 2 * ((m1 >> 4) & 1) + ((m1 >> 1) & 1)) * 128 + (m1 & 1) * 4;
    asm volatile("" : "+v"(mu));
  }
  if (EB == 2) { tb = 128 * cu; asm volatile("" : "+v"(tb) : "s"(rb2)); }
  int rk = XP ? 5 * (cu & 3) : (5 * cu) >> 1;
  const unsigned vk = w < 3 ? l * 16 : OOB;
#define KOF(k) ((k) < 20 ? ((k) + rk >= 20 ? (k) + rk - 20 : (k) + rk) : (k))
#define LDW2T(dd, k) fv[dd][k] = ld4(rw, (k) < 20 ? l * 16 : vk, OFF_W2F + ((2 * cu + ((w & 1) ^ (dd))) * KS2 + k0 + KOF(k)) * 1024);
#define LDW2(k)                                                                                          \
  { const unsigned so_ = OFF_W2F + (2 * cu * KS2 + k0 + KOF(k)) * 1024;                                  \
    fv[0][k] = ld4(rw, (k) < 20 ? l * 16 : vk, so_ + (w & 1) * KS2 * 1024);                              \
    fv[1][k] = ld4(rw, (k) < 20 ? l * 16 : vk, so_ + ((w & 1) ^ 1) * KS2 * 1024); }
  fence();
  {
    f32x4 acc[3][4];
#pragma unroll
    for (int t = 0; t < 3; ++t)
#pragma unroll
      for (int st = 0; st < 4; ++st) acc[t][st] = z4;
    const int f = ((c >> 3) & 1) | ((g & 1) << 1);
    LAS char* rd[4];
#pragma unroll
    for (int st = 0; st < 4; ++st) rd[st] = sm + L_H + (8 * g + (c >> 2)) * 128 + ((st ^ f) * 32) + (c & 3) * 8;
    u32x4 hb[2][4];
#pragma unroll
    for (int st = 0; st < 4; ++st) {
      const u32x2 a0 = tr_read(rd[st]), a1 = tr_read(rd[st] + 512);
      hb[0][st] = u32x4{a0[0], a0[1], a1[0], a1[1]};
    }
#define SB __builtin_amdgcn_sched_barrier(0);
#define G1LOOP(GRAM)                                                                                     \
    _Pragma("unroll") for (int ks = 0; ks < 16; ++ks) {                                                  \
      if (GRAM && ks == 15) { _Pragma("unroll") for (int st = 0; st < 4; ++st) acc[2][st] = mma16(hb[1][st], hb[1][st], acc[2][st]); SB } \
      _Pragma("unroll") for (int st = 0; st < 4; ++st) {                                                 \
        u32x2 a0, a1;                                                                                    \
        acc[0][st] = mma16(wa[0][ks], hb[ks & 1][st], acc[0][st]);                                       \
        SB if (ks < 15) a0 = tr_read(rd[st] + (ks + 1) * 4096); SB                                       \
        acc[1][st] = mma16(wa[1][ks], hb[ks & 1][st], acc[1][st]);                                       \
        SB if (ks < 15) a1 = tr_read(rd[st] + (ks + 1) * 4096 + 512); SB                                 \
        if (!GRAM || ks < 15) acc[2][st] = mma16(GRAM ? hb[ks & 1][st] : wa[2][ks], hb[ks & 1][st], acc[2][st]); \
        else *(LAS f32x4*)(sm + L_TMP + st * 1024 + l * 16) = acc[2][st];                                \
        if (ks < 15) hb[(ks + 1) & 1][st] = u32x4{a0[0], a0[1], a1[0], a1[1]};                           \
        SB                                                                                               \
        if (SL * ks + st == 3 * (16 - NB2) + EP) { if (EM == 2) mu = ld1(rw, mu, 0); if (EB == 2) tb = __builtin_amdgcn_raw_buffer_load_b32(rb2, tb, 0, 0); } \
        if (st < SL) {                                                                                   \
          if (SL * ks + st < 3 * (16 - NB2)) LDW1T((SL * ks + st) % 3, NB2 + (SL * ks + st) / 3)         \
          else if (SL * ks + st - 3 * (16 - NB2) < 2 * NW2) LDW2T((SL * ks + st - 3 * (16 - NB2)) & 1, (SL * ks + st - 3 * (16 - NB2)) >> 1) \
        }                                                                                                \
        SB                                                                                               \
      }                                                                                                  \
    }
    if (w == 3) {
      G1LOOP(true)
    } else {
      G1LOOP(false)
    }
    if (GF) { fence(); gfl = ld1(rw, (cu * 8 + 2 * w + (l & 1)) * 128, OFF_F3 + grp * 8192); fence(); }
    bar();
    STAMP(11);
    if (EF == 0) asm volatile("" :: "v"(tc0), "v"(tc1));
    float rstd[4];
#pragma unroll
    for (int st = 0; st < 4; ++st)
      rstd[st] = __builtin_amdgcn_rsqf(*(LAS float*)(sm + L_TMP + st * 1024 + ((c >> 2) * 16 + c) * 16 + (c & 3) * 4) * (1.f / D_) + EPS);
#pragma unroll
    for (int t = 0; t < 3; ++t) {
      const unsigned ea = e1[t][0], eb = e1[t][1];
      const float bias[4] = {lo16(ea), hi16(ea), lo16(eb), hi16(eb)};
      unsigned gp[4][2];
#pragma unroll
      for (int st = 0; st < 4; ++st) {
        float gv[4];
#pragma unroll
        for (int r = 0; r < 4; ++r) {
          const float pre = acc[t][st][r] * rstd[st] + bias[r];
          const float p = pre > 0.f ? pre : 0.f;
          gv[r] = p * p;
        }
        gp[st][0] = pk(gv[0], gv[1]); gp[st][1] = pk(gv[2], gv[3]);
      }
#pragma unroll
      for (int sp = 0; sp < 2; ++sp) {
        const auto a = __builtin_amdgcn_permlane16_swap(gp[2 * sp][0], gp[2 * sp + 1][0], false, false);
        const auto bq = __builtin_amdgcn_permlane16_swap(gp[2 * sp][1], gp[2 * sp + 1][1], false, false);
        st4(u32x4{a[0], bq[0], a[1], bq[1]}, rw, ((t < 2 || three) ? l * 16 : OOB) + sp * 1024, OFF_G + (grp * KS2 + T0 + t) * 2048);
      }
      if (ES) SB
    }
  }
  fence();
  STAMP(17);
  if (EM == 2) asm volatile("" : "+s"(rk));
  tc2 = ld1(rw, l < (three ? 48 : 32) ? l * 128 : OOB, OFF_G + (grp * KS2 + T0) * 2048);
  const int srow = 64 * grp + 32 * (w >> 1) + n, dcol = 64 * cu + 32 * (w & 1) + 16 * h;
  const int pc8 = l & 7, n2 = 16 * (w & 1) + (l >> 3);
  const unsigned vm = (16 * ((n2 >> 2) & 1) + 4 * (n2 >> 3) + (n2 & 3)) * 512;
  const unsigned sm1 = OFF_MEAN + ((4 * (m1 >> 5) + ((m1 >> 2) & 3)) * 128 + 2 * ((m1 >> 4) & 1) + ((m1 >> 1) & 1)) * 128 + (m1 & 1) * 4;
  unsigned mu2[2] = {0u, 0u};
  u32x4 e20 = {0u, 0u, 0u, 0u};
#undef LDMU
#define LDMU mu2[0] = ld1(rw, vm, sm1); mu2[1] = ld1(rw, vm + 2048, sm1); if (EB == 3) e20 = in4(rb2, (64 * cu + 8 * pc8) * 2, 0);
  if (EM == 0) { LDMU }
  if (EB != 3) e20 = in4(rb2, (64 * cu + 8 * pc8) * 2, 0);
  fence();
  if (GF && any(gfl != tag)) {
#pragma unroll 1
    for (int spin = 0; spin < SPIN; ++spin) {
      asm volatile("" ::: "memory");
      if (!any(ld1(rw, (cu * 8 + 2 * w + (l & 1)) * 128, OFF_F3 + grp * 8192) != tag)) break;
    }
  }
  fence();
  f32x16 acc[4];
#pragma unroll 1
  for (int pass = 0; pass < 64; ++pass) {
#pragma unroll
    for (int t = 0; t < 4; ++t)
#pragma unroll
      for (int r = 0; r < 16; ++r) acc[t][r] = 0.f;
    {
      const int qq = c >> 2;
      LAS char* rb = sm + L_H + (128 * w + (qq & 1) + 8 * (qq >> 1) + 4 * h) * 128 + (c & 3) * 8;
      LAS char* rd[2][2];
#pragma unroll
      for (int ss = 0; ss < 2; ++ss)
#pragma unroll
        for (int r = 0; r < 2; ++r) rd[ss][r] = rb + r * 256 + (((2 * (ss ^ (w >> 1)) + (g & 1)) ^ (r | (qq & 2))) * 32);
      u32x4 hf[2][2], bf[2][2];
#define LDH(ss, ii) { const u32x2 a0 = tr_read(rd[ss][0] + (ii) * 2048), a1 = tr_read(rd[ss][1] + (ii) * 2048); hf[(ii) & 1][ss] = u32x4{a0[0], a0[1], a1[0], a1[1]}; }
#define LDB(ss, ii) bf[(ii) & 1][ss] = *(LAS u32x4*)(sm + L_BAS + w * 16384 + (2 * (ii) + ss) * 1024 + l * 16);
      LDB(0, 0) LDH(0, 0) LDH(1, 0) LDB(1, 0)
#pragma unroll
      for (int ii = 0; ii < 8; ++ii) {
        SB
        acc[0] = mma32(bf[ii & 1][0], hf[ii & 1][0], acc[0]);
        SB if (ii < 7) { LDB(0, ii + 1) LDH(0, ii + 1) } SB
        acc[2] = mma32(bf[ii & 1][0], hf[ii & 1][1], acc[2]);
        SB if (ii < 7) { LDH(1, ii + 1) LDB(1, ii + 1) } SB
        acc[1] = mma32(bf[ii & 1][1], hf[ii & 1][0], acc[1]);
        SB if (NW2 + ii < 22) LDW2(NW2 + ii); SB
        acc[3] = mma32(bf[ii & 1][1], hf[ii & 1][1], acc[3]);
        SB
      }
    }
    fence();
#pragma unroll
    for (int k = NW2 + 8; k < 22; ++k) LDW2(k);
    fence();
    STAMP(12);
    u32x4 fg[2][22];
    const unsigned vg[2] = {l * 16u + kw * 1024u, l * 16u + (kw ^ 1) * 1024u};
#define LDG(ss, k) fg[ss][k] = ld4(rw, ((k) < 20 || w < 3) ? vg[ss] : OOB, OFF_G + (grp * KS2 + k0 + KOF(k)) * 2048);
#pragma unroll 1
    for (int spin = 0; spin < SPIN; ++spin) {
      asm volatile("" ::: "memory");
      const unsigned probe = ld1(rw, l < (w < 3 ? 44 : 40) ? l * 1024 + 1020 : OOB, OFF_G + (grp * KS2 + k0) * 2048);
#pragma unroll
      for (int k = 0; k < GD0; ++k) { LDG(0, k) LDG(1, k) }
      fence();
      if (!any((int)probe < 0)) break;
    }
    if (EF) asm volatile("" :: "v"(tc0), "v"(tc1));
    if (EB == 2) asm volatile("" :: "v"(tb));
    fence();
    STAMP(13);
#pragma unroll
    for (int k = GD0; k < GD; ++k) { LDG(0, k) LDG(1, k) }
    fence();
    unsigned m = 0;
#pragma unroll
    for (int k = 0; k < 22; ++k) {
      acc[0] = mma32(fv[0][k], fg[0][k], acc[0]);
      SB if (k + GD < 22) LDG(0, k + GD) SB
      acc[1] = mma32(fv[1][k], fg[0][k], acc[1]);
      SB
      acc[2] = mma32(fv[0][k], fg[1][k], acc[2]);
      SB if (k + GD < 22) LDG(1, k + GD) if (EM == 3 && k == EK) { LDMU } SB
      acc[3] = mma32(fv[1][k], fg[1][k], acc[3]);
      SB
      m |= fg[0][k][3] | fg[1][k][3];
      SB
    }
    if (!any((int)m < 0)) break;
  }
  asm volatile("" :: "v"(tc2));
  STAMP(14);
#pragma unroll
  for (int j = 0; j < 4; ++j)
#pragma unroll
    for (int qd = 0; qd < 4; ++qd)
      *(LAS f32x4*)(sm + L_BAS + w * 16384 + (w ^ j) * 4096 + qd * 1024 + (l ^ (8 * ((w ^ j) & 1) + 4 * h + 2 * (qd >> 1))) * 16) =
          f32x4{acc[j][4 * qd], acc[j][4 * qd + 1], acc[j][4 * qd + 2], acc[j][4 * qd + 3]};
  bar();
  const rsrc_t ro = mk(out, S_ * D_ * 2);
#pragma unroll
  for (int ii = 0; ii < 2; ++ii) {
    LAS char* src = sm + L_BAS + (2 * (w >> 1) + (pc8 >> 2)) * 4096 + (pc8 & 1) * 2048 + ((n2 + 8 * ii + 32 * ((pc8 >> 1) & 1)) ^ (2 * pc8)) * 16;
    const float muf = __builtin_bit_cast(float, mu2[ii]);
    float o[8];
#pragma unroll
    for (int q = 0; q < 2; ++q) {
      const f32x4 p0 = *(LAS f32x4*)(src + q * 1024), p1 = *(LAS f32x4*)(src + q * 1024 + 16384);
      const f32x4 p2 = *(LAS f32x4*)(src + q * 1024 + 32768), p3 = *(LAS f32x4*)(src + q * 1024 + 49152);
#pragma unroll
      for (int r = 0; r < 4; ++r) o[4 * q + r] = (p0[r] + p1[r]) + (p2[r] + p3[r]) + muf;
    }
#pragma unroll
    for (int e = 0; e < 4; ++e) { o[2 * e] += lo16(e20[e]); o[2 * e + 1] += hi16(e20[e]); }
    __builtin_amdgcn_raw_buffer_store_b128(u32x4{pk(o[0], o[1]), pk(o[2], o[3]), pk(o[4], o[5]), pk(o[6], o[7])}, ro,
                                           ((64 * grp + 32 * (w >> 1) + n2 + 8 * ii) * D_ + 64 * cu + 8 * pc8) * 2, 0, OAUX);
  }
  STAMP(15);
  STAMP_FLUSH
}

extern "C" void kernel_launch(void* const* d_in, const int* in_sizes, int n_in, void* d_out, int out_size, void* d_ws,
                              size_t ws_size, hipStream_t stream) {
  fused<<<256, 256, 0, stream>>>((const u16*)d_in[0], (const u16*)d_in[1], (const u16*)d_in[2], (const u16*)d_in[4], (const u16*)d_in[5],
                                 (const u16*)d_in[6], (const u16*)d_in[7], (char*)d_ws, (u16*)d_out);
}
```
